```python
import torch

def _softmax(x, axis=-1):
    return torch.softmax(x, dim=axis)

def _gelu(x):
    return torch.nn.functional.gelu(x, approximate='tanh')

def _silu(x):
    return torch.nn.functional.silu(x)

def _sigmoid(x):
    return torch.sigmoid(x)

def _layernorm(x, axis=-1, eps=1e-05):
    m = x.mean(dim=axis, keepdim=True)
    v = x.var(dim=axis, keepdim=True, unbiased=False)
    return (x - m) / torch.sqrt(v + eps)

def _scatter_add(tgt, idx, src, axis=0):
    return tgt.clone().index_add_(axis, idx, src)

def _segment_sum(src, seg, n):
    out = torch.zeros((n,) + src.shape[1:], dtype=src.dtype)
    return out.index_add_(0, seg, src)

def _pad(x, pw):
    flat = tuple((v for pair in reversed(pw) for v in pair))
    return torch.nn.functional.pad(x, flat)
kernel_name = 'adaLN_cond__ssm_conv_s4__moe_arch_mod__prompt'

def setup_inputs():

    _w = lambda a: (lambda t: t.to(torch.bfloat16) if t.is_floating_point() else t)(torch.as_tensor(a))

    def _A_setup():
        B, L, D, N = (1, 2048, 448, 256)
        A_re = -(lambda _x: torch.exp(torch.as_tensor(_x)))((lambda *s: torch.randn(*s, dtype=torch.bfloat16) * 12.8)(D, N))
        A_im = torch.pi * (lambda *s: torch.randn(*s, dtype=torch.bfloat16) * 0.05)(D, N)
        B_re = (lambda *s: torch.randn(*s, dtype=torch.bfloat16) * 0.1)(D, N)
        B_im = (lambda *s: torch.randn(*s, dtype=torch.bfloat16) * 0.1)(D, N)
        C_re = (lambda *s: torch.randn(*s, dtype=torch.bfloat16) * 0.1)(D, N)
        C_im = (lambda *s: torch.randn(*s, dtype=torch.bfloat16) * 0.1)(D, N)
        log_dt = (lambda *s: torch.randn(*s, dtype=torch.bfloat16) * 0.05)(D) * 0.5 - 4.0
        Dskip = (lambda *s: torch.randn(*s, dtype=torch.bfloat16) * 0.05)(D)
        return {'u': _w((lambda *s: torch.randn(*s, dtype=torch.bfloat16) * 0.05)(B, L, D)), 'A_re': _w(A_re), 'A_im': _w(A_im), 'B_re': _w(B_re), 'B_im': _w(B_im), 'C_re': _w(C_re), 'C_im': _w(C_im), 'log_dt': _w(log_dt), 'Dskip': _w(Dskip)}
    _A_in = _A_setup()

    def _B_setup():
        B, S, D, F, C = (1, 2048, 448, 1792, 8)
        return {'x': _w((lambda *s: torch.randn(*s, dtype=torch.bfloat16) * 0.05)(B, S, D)), 'w_r': _w((lambda *s: torch.randn(*s, dtype=torch.bfloat16) * 0.05)(D)), 'w_gate': _w((lambda *s: torch.randn(*s, dtype=torch.bfloat16) * 0.05)(D, F)), 'w_up': _w((lambda *s: torch.randn(*s, dtype=torch.bfloat16) * 0.05)(D, F)), 'w_down': _w((lambda *s: torch.randn(*s, dtype=torch.bfloat16) * 0.05)(F, D))}
    _B_in = _B_setup()
    return {'A_u': _A_in['u'], 'A_A_re': _A_in['A_re'], 'A_A_im': _A_in['A_im'], 'A_B_re': _A_in['B_re'], 'A_B_im': _A_in['B_im'], 'A_C_re': _A_in['C_re'], 'A_C_im': _A_in['C_im'], 'A_log_dt': _A_in['log_dt'], 'A_Dskip': _A_in['Dskip'], 'B_x': _B_in['x'], 'B_w_r': _B_in['w_r'], 'B_w_gate': _B_in['w_gate'], 'B_w_up': _B_in['w_up'], 'B_w_down': _B_in['w_down'], 'c': _w((lambda *s: torch.randn(*s, dtype=torch.bfloat16) * 0.1)(1, 448) * 8.0), 'w_mod': _w((lambda *s: torch.randn(*s, dtype=torch.bfloat16) * 0.1)(448, 6 * 448) * 448 ** (-0.5) * 8.0)}

def reference(A_u, A_A_re, A_A_im, A_B_re, A_B_im, A_C_re, A_C_im, A_log_dt, A_Dskip, B_x, B_w_r, B_w_gate, B_w_up, B_w_down, c, w_mod):

    def _A_ref(u, A_re, A_im, B_re, B_im, C_re, C_im, log_dt, Dskip):
        Bsz, L, D = u.shape
        dt = torch.exp(log_dt)[:, None]
        A = A_re + 1j * A_im
        Bc = B_re + 1j * B_im
        Cc = C_re + 1j * C_im
        logdA = dt * A
        dB = dt * Bc
        l = (lambda x, dt: x.to(dt))(torch.arange(L), A_re.dtype)
        P = torch.exp(logdA[:, :, None] * l[None, None, :])
        K = torch.einsum('dn,dnl,dn->dl', Cc, P, dB).real
        L2 = 2 * L
        Kp = _pad(K, ((0, 0), (0, L)))
        up = (lambda x, *d: x.permute(*d))(u, 0, 2, 1)
        up = _pad(up, ((0, 0), (0, 0), (0, L)))
        Kf = (lambda x, axes=(-2, -1): torch.fft.rfft2(x.float(), dim=axes))(Kp[:, None, :], axes=(-2, -1))
        Uf = (lambda x, axes=(-2, -1): torch.fft.rfft2(x.float(), dim=axes))(up[:, :, None, :], axes=(-2, -1))
        Yf = Uf * Kf[None, :, :, :]
        y = (lambda x, s, axes=(-2, -1): torch.fft.irfft2(x, s=s, dim=axes).to(torch.bfloat16))(Yf, (1, L2), axes=(-2, -1))[:, :, 0, :L]
        y = (lambda x, *d: x.permute(*d))(y, 0, 2, 1) + u * Dskip[None, None, :]
        return y

    def _B_ref(x, w_r, w_gate, w_up, w_down):
        B, S, D = x.shape
        C = 8
        r = x @ w_r
        idx = torch.argsort(-r, axis=-1)[:, :C]
        idx_e = (lambda x, n, axis: torch.repeat_interleave(x, n, dim=axis))(idx.reshape(B, C, 1), D, axis=2)
        x_sel = (lambda x, i, axis: torch.take_along_dim(x, i, dim=axis))(x, idx_e, axis=1)
        g = _silu(x_sel @ w_gate)
        u = x_sel @ w_up
        y = g * u @ w_down
        r_sel = _sigmoid((lambda x, i, axis: torch.take_along_dim(x, i, dim=axis))(r, idx, axis=1))
        y = y * r_sel.reshape(B, C, 1)
        b_off = (torch.arange(B) * S).reshape(B, 1)
        flat_idx = (idx + b_off).reshape(B * C)
        zero = (x * 0.0).reshape(B * S, D)
        upd = _scatter_add(zero, flat_idx, y.reshape(B * C, D), axis=0)
        return x + upd.reshape(B, S, D)
    x = A_u
    mod = (_silu(c) @ w_mod).reshape(c.shape[0], 6, 1, -1)
    sa, ca, ga, sb, cb, gb = (mod[:, 0].clone(), mod[:, 1].clone(), mod[:, 2].clone(), mod[:, 3].clone(), mod[:, 4].clone(), mod[:, 5].clone())
    h = _layernorm(x, axis=-1) * (1 + ca) + sa
    x = x + ga * _A_ref(h, A_A_re, A_A_im, A_B_re, A_B_im, A_C_re, A_C_im, A_log_dt, A_Dskip)
    h = _layernorm(x, axis=-1) * (1 + cb) + sb
    x = x + gb * _B_ref(h, B_w_r, B_w_gate, B_w_up, B_w_down)
    return x
```

```cpp
#include <hip/hip_runtime.h>
#include <stdint.h>

typedef unsigned int u32;
typedef unsigned long long u64;
typedef unsigned short u16;
typedef float f32x2 __attribute__((ext_vector_type(2)));
typedef float f32x4 __attribute__((ext_vector_type(4)));
typedef _Float16 f16x2 __attribute__((ext_vector_type(2)));
typedef _Float16 f16x8 __attribute__((ext_vector_type(8)));
typedef __bf16 bf16x2 __attribute__((ext_vector_type(2)));
typedef __bf16 bf16x8 __attribute__((ext_vector_type(8)));
typedef u32 u32x2 __attribute__((ext_vector_type(2)));
typedef u32 u32x4 __attribute__((ext_vector_type(4)));

#define GAS __attribute__((address_space(1)))
#define LAS __attribute__((address_space(3)))
#define RLX_AGENT __ATOMIC_RELAXED, __HIP_MEMORY_SCOPE_AGENT

constexpr int L_ = 2048, D_ = 448, N_ = 256, F_ = 1792;
constexpr int NWG = 256, NT = 512;

constexpr u32 WS_EPOCH = 0;
constexpr u32 WS_STATS = 1024;
constexpr u32 WS_MODB = WS_STATS + 16384;
constexpr u32 WS_RBUF = WS_MODB + 8192;
constexpr u32 WS_ODONE = WS_RBUF + 16384;
constexpr u32 WS_ACT = WS_ODONE + 4096;
constexpr u32 WS_Z = WS_ACT + 28672;
constexpr u32 WS_END = WS_Z + 256 * 448 * 16;
constexpr u32 WS_DBG = 8u << 20;

constexpr int FROW = 1056;
constexpr int FCH = 53 * FROW;
constexpr int LDS_F = 0;
constexpr int KCOPY = 4160;
constexpr int LDS_KC4 = 0;
constexpr int LDS_XCH = 2 * 4 * KCOPY;
constexpr int LDS_TR = LDS_XCH + 16384;
constexpr int TRROW = 912;
constexpr int LDS_KD = 2 * FCH;
constexpr int KDCH = 1312;
constexpr int LDS_HS = LDS_KD + 2 * KDCH;
constexpr int HSCH = 4608;
constexpr int LDS_XS = LDS_HS + 2 * HSCH;
constexpr int LDS_MISC = LDS_XS + 2 * 4096;
constexpr int LDS_TOTAL = LDS_MISC + 2048;
constexpr int MLP_H = 116 * 1024;
constexpr int MLP_X1 = MLP_H + 8 * TRROW;
constexpr int MLP_RED = MLP_X1 + 256;
constexpr int MLP_TOP = MLP_RED + 4096;
static_assert(MLP_TOP + 1024 <= LDS_MISC, "lds");
static_assert(LDS_TR + 8 * TRROW <= 2 * FCH, "lds");
static_assert(LDS_TOTAL <= 160 * 1024, "lds");
static_assert(LDS_TOTAL > 80 * 1024, "one workgroup per CU");

constexpr u32 SPIN_CAP = 1u << 14;

struct KTab { u16 v[2064]; };
constexpr KTab make_ktab() {
    KTab t{};
    for (int x = 0; x < 2064; ++x) {
        int l = 2047 - x, idx = 641;
        if (l >= 0) {
            int k = l < 256 ? 0 : l < 512 ? 1 : l < 1024 ? 2 : 3;
            int q = l >> k, rem = l & ((1 << k) - 1), half = (1 << k) >> 1;
            if (k > 0 && (rem > half || (rem == half && (q & 1)))) q += 1;
            idx = 128 * k + q;
        }
        t.v[x] = (u16)(2 * idx);
    }
    return t;
}
__device__ const KTab g_ktab __attribute__((aligned(16))) = make_ktab();

struct Params {
    const u16 *x, *a_re, *a_im, *b_re, *b_im, *c_re, *c_im, *log_dt, *dskip, *w_r, *w_gate, *w_up, *w_down, *c, *w_mod;
    u16* out;
    unsigned char* ws;
};

__device__ __forceinline__ float bflo(u32 u) { return __uint_as_float(u << 16); }
__device__ __forceinline__ float bfhi(u32 u) { return __uint_as_float(u & 0xffff0000u); }
__device__ __forceinline__ u32 pkbf(f32x2 a) { return __builtin_bit_cast(u32, __builtin_convertvector(a, bf16x2)); }
__device__ __forceinline__ u32 pkh(f32x2 a) { return __builtin_bit_cast(u32, __builtin_convertvector(a, f16x2)); }
__device__ __forceinline__ f32x2 unbf(u32 u) { return f32x2{bflo(u), bfhi(u)}; }
__device__ __forceinline__ f32x2 rbf2(f32x2 a) { return unbf(pkbf(a)); }
__device__ __forceinline__ float rbf(float a) { return bflo(pkbf(f32x2{a, 0.f})); }
__device__ __forceinline__ f32x2 cmul(f32x2 a, f32x2 b, f32x2 brot) {
    return __builtin_elementwise_fma(a.yy, brot, a.xx * b);
}
template <int CTRL> __device__ __forceinline__ float dppf(float x) {
    return __builtin_bit_cast(float, __builtin_amdgcn_mov_dpp(__builtin_bit_cast(int, x), CTRL, 0xf, 0xf, true));
}
template <int CTRL> __device__ __forceinline__ u32 dppu(u32 x) {
    return (u32)__builtin_amdgcn_mov_dpp((int)x, CTRL, 0xf, 0xf, true);
}
constexpr int XOR1 = 0xB1, XOR2 = 0x4E, HMIR = 0x141, MIR = 0x140;
__device__ __forceinline__ float row_sum(float x) {
    x += dppf<XOR1>(x); x += dppf<XOR2>(x); x += dppf<HMIR>(x); x += dppf<MIR>(x);
    return x;
}
__device__ __forceinline__ float wave_sum(float x) {
    x = row_sum(x);
    auto s = __builtin_amdgcn_permlane16_swap(__float_as_uint(x), __float_as_uint(x), false, false);
    x = __uint_as_float(s[0]) + __uint_as_float(s[1]);
    auto t = __builtin_amdgcn_permlane32_swap(__float_as_uint(x), __float_as_uint(x), false, false);
    return __uint_as_float(t[0]) + __uint_as_float(t[1]);
}
__device__ __forceinline__ u32 umax(u32 a, u32 b) { return a > b ? a : b; }
__device__ __forceinline__ u32 wave_umax(u32 x) {
    x = umax(x, dppu<XOR1>(x)); x = umax(x, dppu<XOR2>(x)); x = umax(x, dppu<HMIR>(x)); x = umax(x, dppu<MIR>(x));
    auto s = __builtin_amdgcn_permlane16_swap(x, x, false, false);
    x = umax(s[0], s[1]);
    auto t = __builtin_amdgcn_permlane32_swap(x, x, false, false);
    return umax(t[0], t[1]);
}
__device__ __forceinline__ float pow2i(int e) {
    e = e > 127 ? 127 : e;
    return e < -126 ? 0.f : __uint_as_float((u32)(e + 127) << 23);
}
__device__ __forceinline__ float sigmoidf_(float x) { return 1.f / (1.f + __expf(-x)); }

__device__ __forceinline__ void row_stats(const f32x2 (&v)[4], bool active, float& m, float& den) {
    f32x2 s2 = (v[0] + v[1]) + (v[2] + v[3]);
    const float mean = wave_sum(s2.x + s2.y) * (1.f / D_);
    f32x2 q2 = {0.f, 0.f};
#pragma unroll
    for (int i = 0; i < 4; ++i) { const f32x2 dlt = v[i] - mean; q2 = __builtin_elementwise_fma(dlt, dlt, q2); }
    const float var = wave_sum(active ? q2.x + q2.y : 0.f) * (1.f / D_);
    m = rbf(mean);
    den = rbf(__builtin_sqrtf(rbf(rbf(var) + 1e-5f)));
}

#define STAMP(i) do { } while (0)

template <int QS>
__device__ __forceinline__ void conv_v(const LAS unsigned char* pa, const LAS unsigned char* pa2, const LAS unsigned char* pb,
                                       f32x4 (&acc)[4]) {
    constexpr int Q[4] = {QS ? 1 : 0, QS ? 2 : 3, QS ? 4 : 5, QS ? 7 : 6};
    constexpr int QM = Q[3];
    f16x8 A[8], B[8];
#pragma unroll
    for (int i = 0; i <= QM; ++i) {
        const u32x2 lo = *(const LAS u32x2*)(pa + 512 * (7 - i));
        const u32x2 hi = *(const LAS u32x2*)(pa2 + 512 * (7 - i));
        A[i] = __builtin_bit_cast(f16x8, u32x4{lo.x, lo.y, hi.x, hi.y});
        B[i] = *(const LAS f16x8*)(pb + 512 * i);
    }
#pragma unroll
    for (int a = 0; a <= QM; ++a) {
#pragma unroll
        for (int k = 0; k < 4; ++k)
            if (a <= Q[k]) acc[k] = __builtin_amdgcn_mfma_f32_16x16x32_f16(A[Q[k] - a], B[a], acc[k], 0, 0, 0);
    }
}

__global__ void __launch_bounds__(NT, 2) fused_block(Params p) {
    __shared__ __attribute__((aligned(16))) unsigned char smem_[LDS_TOTAL];
    LAS unsigned char* const smem = (LAS unsigned char*)smem_;
    const int tid = threadIdx.x, lane = tid & 63;
    const int wave = __builtin_amdgcn_readfirstlane(tid >> 6);
    const int bid = blockIdx.x;
    const int xg = bid & 7, xi = bid >> 3;
    const bool is_conv = xi < 28;
    const int pair = xg * 28 + xi, d0 = 2 * pair;
    const int sid = xg * 4 + (xi - 28);
    const bool is_mlp = !is_conv && sid < 28;
    const __amdgpu_buffer_rsrc_t wsr = __builtin_amdgcn_make_buffer_rsrc(p.ws, 0, WS_END, 0x00020000);
    const int r16 = lane & 15, g4 = lane >> 4;
    STAMP(0);

    const int town = 8 * bid + wave;
    {
        const u32x4 ones = {~0u, ~0u, ~0u, ~0u};
        if (is_conv) __builtin_amdgcn_raw_buffer_store_b128(ones, wsr, WS_Z + (u32)(((tid >> 1) * D_ + d0 + (tid & 1)) * 16), 0, 16);
        if (is_mlp && tid < 64) __builtin_amdgcn_raw_buffer_store_b128(ones, wsr, WS_ACT + (u32)((tid >> 3) * (2 * F_) + sid * 128 + (tid & 7) * 16), 0, 16);
        if (lane == 0) __hip_atomic_store((GAS u64*)(p.ws + WS_RBUF) + town, ~0ull, RLX_AGENT);
    }
    asm volatile("" ::: "memory");
    const u32 tag = __hip_atomic_load((GAS u32*)(p.ws + WS_EPOCH), RLX_AGENT) + 1u;
    const bool act56 = lane < 56;
    u32x4 xrow = {0u, 0u, 0u, 0u};
    if (act56) xrow = *(const GAS u32x4*)(p.x + town * D_ + lane * 8);
    asm volatile("" ::: "memory");

    const int ch = wave >> 2;
    u16 q_are = 0, q_aim = 0, q_bre = 0, q_bim = 0, q_cre = 0, q_cim = 0, q_ldt = 0, q_c = 0;
    u32 wm[6] = {0, 0, 0, 0, 0, 0}, xc[4] = {0, 0, 0, 0};
    u32x4 kt0 = {0, 0, 0, 0}; u32x2 kt1 = {0, 0};
    if (is_conv) {
        const int o = d0 * N_ + tid;
        q_are = p.a_re[o]; q_aim = p.a_im[o]; q_bre = p.b_re[o]; q_bim = p.b_im[o]; q_cre = p.c_re[o]; q_cim = p.c_im[o];
        q_ldt = p.log_dt[d0 + ch];
        if (tid < D_) {
            q_c = p.c[tid];
#pragma unroll
            for (int g = 0; g < 6; ++g) wm[g] = *(const GAS u32*)(p.w_mod + tid * (6 * D_) + g * D_ + d0);
        }
#pragma unroll
        for (int i = 0; i < 4; ++i) xc[i] = *(const GAS u32*)(p.x + (4 * tid + i) * D_ + d0);
        kt0 = *(const u32x4*)(g_ktab.v + 8 * (tid & 255));
        kt1 = *(const u32x2*)(g_ktab.v + 8 * (tid & 255) + 8);
    }
    asm volatile("" ::: "memory");
    {
        const f32x2 v[4] = {unbf(xrow.x), unbf(xrow.y), unbf(xrow.z), unbf(xrow.w)};
        float m, den;
        row_stats(v, act56, m, den);
        if (lane == 0)
            __hip_atomic_store((GAS u64*)(p.ws + WS_STATS) + town,
                               ((u64)tag << 32) | (u64)((__float_as_uint(den) & 0xffff0000u) | (__float_as_uint(m) >> 16)), RLX_AGENT);
    }
    STAMP(1);

    LAS float* const misc = (LAS float*)(smem + LDS_MISC);
    f16x8 wgu[14];
    f16x8 wdn[7];

    if (is_conv) {
        if (wave < 7) {
            const float cv = bflo(q_c);
            const float s = rbf(cv * sigmoidf_(cv));
#pragma unroll
            for (int g = 0; g < 6; ++g) {
                const f32x2 pr = unbf(wm[g]) * s;
                const float a = row_sum(pr.x), b = row_sum(pr.y);
                if (r16 == 0) *(LAS f32x2*)(misc + 64 + (wave * 4 + g4) * 12 + 2 * g) = f32x2{a, b};
            }
        }
        if (tid < 64) *(LAS u32x4*)(smem + LDS_HS + (tid >> 5) * HSCH + (tid & 31) * 16) = u32x4{0u, 0u, 0u, 0u};
        if (tid >= 64 && tid < 66) *(LAS u16*)(smem + LDS_KD + (tid - 64) * KDCH + 2 * 641) = 0;

        const float dt = rbf(expf(bflo(q_ldt)));
        float lr = dt * bflo(q_are);
        const float li = dt * bflo(q_aim);
        const float bre = dt * bflo(q_bre), bim = dt * bflo(q_bim);
        const float cre = bflo(q_cre), cim = bflo(q_cim);
        const float cr = cre * bre - cim * bim, ci = cre * bim + cim * bre;
        const float mw = __uint_as_float(wave_umax(umax(__float_as_uint(cr) & 0x7fffffffu, __float_as_uint(ci) & 0x7fffffffu)));
        const int ew = __builtin_amdgcn_frexp_expf(mw);
        float gw = 0.f;
        if (__any(lr > 0.f)) {
            gw = __uint_as_float(wave_umax(__float_as_uint(fmaxf(lr, 0.f))));
            lr -= gw;
        }
        if (lane == 0) { ((LAS int*)misc)[wave] = ew; misc[8 + wave] = gw; }
        const float a2 = lr * 1.44269504088896341f;
        const float ph = li * 0.15915494309189535f;
        const float pe = fmaf(li, 6.4206383e-09f, fmaf(li, 0.15915494309189535f, -ph));
        auto anchor = [&](float m) {
            const float mag = __builtin_amdgcn_exp2f(a2 * m);
            const float rev = fmaf(pe, m, __builtin_amdgcn_fractf(ph * m));
            return f32x2{__builtin_amdgcn_cosf(rev), __builtin_amdgcn_sinf(rev)} * mag;
        };
        const f32x2 e1 = anchor(1.f), e256 = anchor(256.f);
        f32x2 e8 = anchor(8.f), e64 = anchor(64.f);
        e8.y = -e8.y; e64.y = -e64.y;
        const f32x2 e1r = {-e1.y, e1.x}, e256r = {-e256.y, e256.x}, e8r = {-e8.y, e8.x}, e64r = {-e64.y, e64.x};
        LAS unsigned char* const fw = smem + LDS_F + ch * FCH + (tid & 255) * 4;
        auto put = [&](int row, f32x2 v) { *(LAS u32*)(fw + row * FROW) = pkh(v); };
        {
            f32x2 s[8];
            s[0] = f32x2{32768.f, 0.f};
            s[1] = e1 * 32768.f;
#pragma unroll
            for (int e = 2; e < 8; ++e) s[e] = cmul(s[e - 1], e1, e1r);
#pragma unroll
            for (int e = 0; e < 8; ++e) put(e, s[e]);
            f32x2 u0 = e256 * 32768.f, u2 = cmul(s[2], e256, e256r), u4 = cmul(s[4], e256, e256r), u6 = cmul(s[6], e256, e256r);
            put(8, u0); put(9, u2); put(10, u4); put(11, u6);
            u0 = cmul(u0, e256, e256r); u4 = cmul(u4, e256, e256r); put(12, u0); put(13, u4);
            u0 = cmul(u0, e256, e256r); u4 = cmul(u4, e256, e256r); put(14, u0); put(15, u4);
#pragma unroll
            for (int i = 16; i < 21; ++i) { u0 = cmul(u0, e256, e256r); put(i, u0); }
        }
        {
            f32x2 b[4];
            b[0] = f32x2{__builtin_amdgcn_ldexpf(cr, 14 - ew), -__builtin_amdgcn_ldexpf(ci, 14 - ew)};
#pragma unroll
            for (int q = 1; q < 4; ++q) b[q] = cmul(b[q - 1], e64, e64r);
#pragma unroll
            for (int j = 0; j < 8; ++j) {
#pragma unroll
                for (int q = 0; q < 4; ++q) {
                    put(21 + 8 * q + j, b[q]);
                    if (j < 7) b[q] = cmul(b[q], e8, e8r);
                }
            }
        }
        __syncthreads();
        STAMP(2);

        if (wave == 7 && lane < 12) {
            float s = 0.f;
#pragma unroll
            for (int i = 0; i < 28; ++i) s += misc[64 + i * 12 + lane];
            s = rbf(s);
            misc[16 + lane] = s;
            const u32 other = dppu<XOR1>(__float_as_uint(s));
            if (lane >= 6 && !(lane & 1))
                __hip_atomic_store((GAS u64*)(p.ws + WS_MODB) + ((lane - 6) >> 1) * 224 + pair,
                                   ((u64)tag << 32) | (u64)((other & 0xffff0000u) | (__float_as_uint(s) >> 16)), RLX_AGENT);
        }

        int kexp;
        {
            const int ta = (wave >> 1) & 1, tb = wave & 1;
            const LAS unsigned char* pa = smem + LDS_F + ch * FCH + (16 * ta + r16) * FROW + g4 * 16;
            const LAS unsigned char* pb = smem + LDS_F + ch * FCH + (21 + 16 * tb + r16) * FROW + g4 * 16;
            f32x4 acc[4];
#pragma unroll
            for (int w4 = 0; w4 < 4; ++w4) {
                acc[w4] = f32x4{0.f, 0.f, 0.f, 0.f};
#pragma unroll
                for (int k4 = 0; k4 < 4; ++k4) {
                    const int ks = 4 * w4 + k4;
                    acc[w4] = __builtin_amdgcn_mfma_f32_16x16x32_f16(*(const LAS f16x8*)(pa + 64 * ks), *(const LAS f16x8*)(pb + 64 * ks), acc[w4], 0, 0, 0);
                }
            }
            int ews[4]; float gws[4];
#pragma unroll
            for (int w4 = 0; w4 < 4; ++w4) { ews[w4] = ((LAS int*)misc)[4 * ch + w4]; gws[w4] = misc[8 + 4 * ch + w4]; }
            const int emax = max(max(ews[0], ews[1]), max(ews[2], ews[3]));
            const float gmax = fmaxf(fmaxf(gws[0], gws[1]), fmaxf(gws[2], gws[3]));
            const int col = 16 * tb + r16;
            int ibase, o1, o2;
            if (ta == 0) {
                ibase = g4 < 2 ? 4 * g4 + 8 * col : g4 == 2 ? 256 + 4 * col : 384 + 2 * col;
                o1 = 1; o2 = g4 == 3 ? 64 : 2;
            } else { ibase = g4 == 0 ? 512 + col : 640; o1 = 32; o2 = 64; }
            f32x4 val = {0.f, 0.f, 0.f, 0.f};
            kexp = emax - 6;
            if (__builtin_amdgcn_readfirstlane(gmax > 0.f)) {
                const float G = ceilf(gmax * (2048.f * 1.44269504088896341f));
                kexp += (int)fminf(G, 1000.f);
#pragma unroll
                for (int reg = 0; reg < 4; ++reg) {
                    const int i = ibase + (reg & 1) * o1 + (reg >> 1) * o2;
                    const int k = i < 256 ? 0 : i < 384 ? 1 : i < 512 ? 2 : 3;
                    const float lp = (float)((i - 128 * k) << k);
#pragma unroll
                    for (int w4 = 0; w4 < 4; ++w4)
                        val[reg] = fmaf(acc[w4][reg] * pow2i(ews[w4] - emax - 23),
                                        __builtin_amdgcn_exp2f(fmaf(gws[w4] * 1.44269504088896341f, lp, -G)), val[reg]);
                }
            } else {
#pragma unroll
                for (int w4 = 0; w4 < 4; ++w4) val = __builtin_elementwise_fma(acc[w4], (f32x4)pow2i(ews[w4] - emax - 23), val);
            }
            LAS _Float16* kd = (LAS _Float16*)(smem + LDS_KD + ch * KDCH);
            if (ta == 0 || g4 == 0) {
#pragma unroll
                for (int reg = 0; reg < 4; ++reg) kd[ibase + (reg & 1) * o1 + (reg >> 1) * o2] = (_Float16)val[reg];
            } else if (g4 == 1 && col == 0) kd[640] = (_Float16)val[0];
        }
        __syncthreads();
        STAMP(3);

        {
            const LAS unsigned char* kd = smem + LDS_KD + ch * KDCH;
            const u32 tb[6] = {kt0.x, kt0.y, kt0.z, kt0.w, kt1.x, kt1.y};
            u32 rg[6];
#pragma unroll
            for (int i = 0; i < 6; ++i) {
                const u32 lo = *(const LAS u16*)(kd + (tb[i] & 0xffffu));
                const u32 hi = *(const LAS u16*)(kd + (tb[i] >> 16));
                rg[i] = lo | (hi << 16);
            }
            u32 al[5];
#pragma unroll
            for (int i = 0; i < 5; ++i) al[i] = __builtin_amdgcn_alignbit(rg[i + 1], rg[i], 16);
            LAS unsigned char* kc = smem + LDS_KC4 + ch * 4 * KCOPY + (tid & 255) * 16;
            *(LAS u32x4*)(kc) = u32x4{rg[0], rg[1], rg[2], rg[3]};
            *(LAS u32x4*)(kc + KCOPY) = u32x4{al[0], al[1], al[2], al[3]};
            *(LAS u32x4*)(kc + 2 * KCOPY) = u32x4{rg[1], rg[2], rg[3], rg[4]};
            *(LAS u32x4*)(kc + 3 * KCOPY) = u32x4{al[1], al[2], al[3], al[4]};
            if ((tid & 255) < 8)
                *(LAS u32x4*)(smem + LDS_KC4 + (ch * 4 + ((tid & 255) >> 1)) * KCOPY + 4096 + (tid & 1) * 16) = u32x4{0u, 0u, 0u, 0u};
        }
        STAMP(4);

        u32x4 st0, st1;
        for (u32 spins = 0;; ++spins) {
            asm volatile("" ::: "memory");
            st0 = __builtin_amdgcn_raw_buffer_load_b128(wsr, WS_STATS + (u32)tid * 32u, 0, 16);
            st1 = __builtin_amdgcn_raw_buffer_load_b128(wsr, WS_STATS + (u32)tid * 32u + 16u, 0, 16);
            const bool ok = st0.y == tag && st0.w == tag && st1.y == tag && st1.w == tag;
            if (__all(ok) || spins > SPIN_CAP) break;
            __builtin_amdgcn_s_sleep(1);
        }
        STAMP(5);
        int hexp[2];
        {
            const f32x2 m01 = {bflo(st0.x), bflo(st0.z)}, m23 = {bflo(st1.x), bflo(st1.z)};
            const f32x2 rd01 = {__builtin_amdgcn_rcpf(bfhi(st0.x)), __builtin_amdgcn_rcpf(bfhi(st0.z))};
            const f32x2 rd23 = {__builtin_amdgcn_rcpf(bfhi(st1.x)), __builtin_amdgcn_rcpf(bfhi(st1.z))};
#pragma unroll
            for (int c2 = 0; c2 < 2; ++c2) {
                const float sa = misc[16 + c2], ca1 = rbf(1.f + misc[18 + c2]);
                const float hmax = fmaf(66.f, fabsf(ca1), fabsf(sa) * 1.02f);
                hexp[c2] = __builtin_amdgcn_frexp_expf(hmax) - 15;
                const f32x2 x01 = c2 ? f32x2{bfhi(xc[0]), bfhi(xc[1])} : f32x2{bflo(xc[0]), bflo(xc[1])};
                const f32x2 x23 = c2 ? f32x2{bfhi(xc[2]), bfhi(xc[3])} : f32x2{bflo(xc[2]), bflo(xc[3])};
                f32x2 h01 = rbf2(rbf2(rbf2(rbf2(x01 - m01) * rd01) * ca1) + sa);
                f32x2 h23 = rbf2(rbf2(rbf2(rbf2(x23 - m23) * rd23) * ca1) + sa);
                h01 = f32x2{__builtin_amdgcn_ldexpf(h01.x, -hexp[c2]), __builtin_amdgcn_ldexpf(h01.y, -hexp[c2])};
                h23 = f32x2{__builtin_amdgcn_ldexpf(h23.x, -hexp[c2]), __builtin_amdgcn_ldexpf(h23.y, -hexp[c2])};
                *(LAS u32x2*)(smem + LDS_HS + c2 * HSCH + 512 + tid * 8) = u32x2{pkh(h01), pkh(h23)};
                const u32 xa = c2 ? __builtin_amdgcn_perm(xc[1], xc[0], 0x07060302u) : __builtin_amdgcn_perm(xc[1], xc[0], 0x05040100u);
                const u32 xb = c2 ? __builtin_amdgcn_perm(xc[3], xc[2], 0x07060302u) : __builtin_amdgcn_perm(xc[3], xc[2], 0x05040100u);
                *(LAS u32x2*)(smem + LDS_XS + c2 * 4096 + tid * 8) = u32x2{xa, xb};
            }
        }
        __syncthreads();
        STAMP(6);
        if (bid == 0 && tid == 0) __hip_atomic_store((GAS u32*)(p.ws + WS_EPOCH), tag, RLX_AGENT);

        const int vh = (wave >> 1) & 1, qs = wave & 1;
        f32x4 acc[4];
#pragma unroll
        for (int k = 0; k < 4; ++k) acc[k] = f32x4{0.f, 0.f, 0.f, 0.f};
        {
            const LAS unsigned char* pa = smem + LDS_KC4 + (ch * 4 + 3 - (r16 & 3)) * KCOPY
                                          + 2 * (2044 - 256 * 7 - 16 * (8 * vh + 1) - 4 * (r16 >> 2) + 8 * g4);
            const LAS unsigned char* pb = smem + LDS_HS + ch * HSCH + 512 - 32 * (8 * vh + 1) + 32 * r16 + 16 * g4;
            const LAS unsigned char* pa2 = pa + 8;
            asm volatile("" : "+v"(pa2));
#pragma unroll 1
            for (int vi = 0; vi < 4; ++vi) {
                if (qs) conv_v<1>(pa, pa2, pb, acc); else conv_v<0>(pa, pa2, pb, acc);
                pa -= 64; pa2 -= 64; pb -= 64;
            }
        }
        STAMP(7);
        {
            LAS f32x4* xw = (LAS f32x4*)(smem + LDS_XCH + wave * 2048) + lane;
            xw[0] = vh ? acc[0] : acc[2];
            xw[64] = vh ? acc[1] : acc[3];
        }
        __syncthreads();
        {
            const LAS f32x4* xr = (const LAS f32x4*)(smem + LDS_XCH + (wave ^ 2) * 2048) + lane;
            const f32x4 o0 = xr[0], o1 = xr[64];
            const f32x4 ya = vh ? o0 + acc[2] : acc[0] + o0;
            const f32x4 yb = vh ? o1 + acc[3] : acc[1] + o1;
            const int qa = qs ? (vh ? 4 : 1) : (vh ? 5 : 0), qb = qs ? (vh ? 7 : 2) : (vh ? 6 : 3);
            const int he = ch ? hexp[1] : hexp[0];
            const int ye = kexp + he;
            const float dsk = bflo(p.dskip[d0 + ch]), ga = misc[20 + ch];
            u32x2 res[2];
#pragma unroll
            for (int i = 0; i < 2; ++i) {
                const int t0 = 16 * (16 * (i ? qb : qa) + r16) + 4 * g4;
                const f32x4 y = i ? yb : ya;
                const u32x2 hh = *(const LAS u32x2*)(smem + LDS_HS + ch * HSCH + 512 + 2 * t0);
                const u32x2 xx = *(const LAS u32x2*)(smem + LDS_XS + ch * 4096 + 2 * t0);
#pragma unroll
                for (int k = 0; k < 2; ++k) {
                    const f32x2 yc = rbf2(f32x2{__builtin_amdgcn_ldexpf(y[2 * k], ye), __builtin_amdgcn_ldexpf(y[2 * k + 1], ye)});
                    const u32 hk = hh[k];
                    const f32x2 h16 = __builtin_convertvector(__builtin_bit_cast(f16x2, hk), f32x2);
                    const f32x2 hv = {__builtin_amdgcn_ldexpf(h16.x, he), __builtin_amdgcn_ldexpf(h16.y, he)};
                    const f32x2 yy = rbf2(yc + rbf2(hv * dsk));
                    res[i][k] = pkbf(unbf(xx[k]) + rbf2(yy * ga));
                }
            }
            const auto s0 = __builtin_amdgcn_permlane16_swap(res[0].x, res[1].x, false, false);
            const auto s1 = __builtin_amdgcn_permlane16_swap(res[0].y, res[1].y, false, false);
            const int tg = 2 * (16 * ((g4 & 1) ? qb : qa) + r16) + (g4 >> 1);
            __builtin_amdgcn_raw_buffer_store_b128(u32x4{s0[0], s1[0], s0[1], s1[1]}, wsr, WS_Z + (u32)((tg * D_ + d0 + ch) * 16), 0, 16);
        }
        STAMP(8);
    } else {
        if (is_mlp) {
#pragma unroll
            for (int i = 0; i < 7; ++i) {
                const int cidx = tid + NT * i, k = cidx >> 3, c8 = cidx & 7;
                const u32x4 a = *(const GAS u32x4*)(p.w_gate + k * F_ + 64 * sid + 8 * c8);
                const u32x4 b = *(const GAS u32x4*)(p.w_up + k * F_ + 64 * sid + 8 * c8);
                *(LAS u32x4*)(smem + cidx * 16) = a;
                *(LAS u32x4*)(smem + 57344 + 16 + cidx * 16) = b;
            }
            __syncthreads();
            {
                const LAS unsigned char* src = smem + (r16 >> 3) * (57344 + 16) + (8 * wave + (r16 & 7)) * 2 + g4 * 8 * 128;
#pragma unroll
                for (int ks = 0; ks < 14; ++ks) {
                    u32 w[4];
#pragma unroll
                    for (int e = 0; e < 4; ++e) {
                        const u32 lo = *(const LAS u16*)(src + (32 * ks + 2 * e) * 128);
                        const u32 hi = *(const LAS u16*)(src + (32 * ks + 2 * e + 1) * 128);
                        w[e] = lo | (hi << 16);
                    }
                    wgu[ks] = __builtin_bit_cast(f16x8, u32x4{w[0], w[1], w[2], w[3]});
                }
            }
            __syncthreads();
#pragma unroll
            for (int i = 0; i < 7; ++i) {
                const int cidx = tid + NT * i, f = cidx >> 1, c8 = cidx & 1;
                *(LAS u32x4*)(smem + cidx * 16) = *(const GAS u32x4*)(p.w_down + f * D_ + 16 * sid + 8 * c8);
            }
            __syncthreads();
            {
                const LAS unsigned char* src = smem + (224 * wave + 8 * g4) * 32 + r16 * 2;
#pragma unroll
                for (int ks = 0; ks < 7; ++ks) {
                    u32 w[4];
#pragma unroll
                    for (int e = 0; e < 4; ++e) {
                        const u32 lo = *(const LAS u16*)(src + (32 * ks + 2 * e) * 32);
                        const u32 hi = *(const LAS u16*)(src + (32 * ks + 2 * e + 1) * 32);
                        w[e] = lo | (hi << 16);
                    }
                    wdn[ks] = __builtin_bit_cast(f16x8, u32x4{w[0], w[1], w[2], w[3]});
                }
            }
        }
        for (u32 spins = 0;; ++spins) {
            asm volatile("" ::: "memory");
            const u32x4 st0 = __builtin_amdgcn_raw_buffer_load_b128(wsr, WS_STATS + (u32)tid * 32u, 0, 16);
            const u32x4 st1 = __builtin_amdgcn_raw_buffer_load_b128(wsr, WS_STATS + (u32)tid * 32u + 16u, 0, 16);
            const bool ok = st0.y == tag && st0.w == tag && st1.y == tag && st1.w == tag;
            if (__all(ok) || spins > SPIN_CAP) break;
            __builtin_amdgcn_s_sleep(1);
        }
        __syncthreads();
        STAMP(8);
    }

    f32x2 sb[4], cb1[4], gb[4], wr[4];
    {
        u32x4 mb[6];
        const int l56 = act56 ? lane : 0;
        for (u32 spins = 0;; ++spins) {
            asm volatile("" ::: "memory");
            bool ok = true;
#pragma unroll
            for (int g = 0; g < 3; ++g) {
                mb[2 * g] = __builtin_amdgcn_raw_buffer_load_b128(wsr, WS_MODB + (u32)((g * 224 + 4 * l56) * 8), 0, 16);
                mb[2 * g + 1] = __builtin_amdgcn_raw_buffer_load_b128(wsr, WS_MODB + (u32)((g * 224 + 4 * l56) * 8 + 16), 0, 16);
            }
#pragma unroll
            for (int i = 0; i < 6; ++i) ok = ok && mb[i].y == tag && mb[i].w == tag;
            if (__all(ok) || spins > SPIN_CAP) break;
            __builtin_amdgcn_s_sleep(1);
        }
        const u32x4 wrv = *(const GAS u32x4*)(p.w_r + 8 * l56);
#pragma unroll
        for (int i = 0; i < 4; ++i) {
            sb[i] = unbf((i & 1) ? mb[i >> 1].z : mb[i >> 1].x);
            cb1[i] = rbf2(unbf((i & 1) ? mb[2 + (i >> 1)].z : mb[2 + (i >> 1)].x) + 1.f);
            gb[i] = unbf((i & 1) ? mb[4 + (i >> 1)].z : mb[4 + (i >> 1)].x);
            wr[i] = unbf(wrv[i]);
        }
    }
    STAMP(9);
    {
        u32x4 z = {0u, 0u, 0u, 0u};
        if (wave < 7) {
            for (u32 spins = 0;; ++spins) {
                asm volatile("" ::: "memory");
                z = __builtin_amdgcn_raw_buffer_load_b128(wsr, WS_Z + (u32)((bid * D_ + tid) * 16), 0, 16);
                bool ok = true;
#pragma unroll
                for (int i = 0; i < 4; ++i) ok = ok && (z[i] & 0xffffu) != 0xffffu && (z[i] >> 16) != 0xffffu;
                if (__all(ok) || spins > SPIN_CAP) break;
                __builtin_amdgcn_s_sleep(1);
            }
            LAS unsigned char* tr = smem + LDS_TR + tid * 2;
#pragma unroll
            for (int i = 0; i < 4; ++i) {
                *(LAS u16*)(tr + (2 * i) * TRROW) = (u16)z[i];
                *(LAS u16*)(tr + (2 * i + 1) * TRROW) = (u16)(z[i] >> 16);
            }
        }
    }
    __syncthreads();
    STAMP(10);
    {
        u32x4 xr = {0u, 0u, 0u, 0u};
        if (act56) xr = *(const LAS u32x4*)(smem + LDS_TR + wave * TRROW + lane * 16);
        const f32x2 v[4] = {unbf(xr.x), unbf(xr.y), unbf(xr.z), unbf(xr.w)};
        float m, den;
        row_stats(v, act56, m, den);
        const float rd = __builtin_amdgcn_rcpf(den);
        f32x2 h2[4], racc = {0.f, 0.f};
#pragma unroll
        for (int i = 0; i < 4; ++i) {
            h2[i] = rbf2(rbf2(rbf2(rbf2(v[i] - m) * rd) * cb1[i]) + sb[i]);
            racc = __builtin_elementwise_fma(h2[i], wr[i], racc);
        }
        const float r = rbf(wave_sum(act56 ? racc.x + racc.y : 0.f));
        if (lane == 0)
            __hip_atomic_store((GAS u64*)(p.ws + WS_RBUF) + town,
                               ((u64)(__float_as_uint(den) >> 16) << 32) | (u64)((__float_as_uint(m) & 0xffff0000u) | (__float_as_uint(r) >> 16)),
                               RLX_AGENT);
        u32x4 o;
#pragma unroll
        for (int i = 0; i < 4; ++i) o[i] = pkbf(v[i] + rbf2(h2[i] * gb[i]));
        const __amdgpu_buffer_rsrc_t outr = __builtin_amdgcn_make_buffer_rsrc(p.out, 0, L_ * D_ * 2, 0x00020000);
        if (act56) __builtin_amdgcn_raw_buffer_store_b128(o, outr, (u32)((town * D_ + lane * 8) * 2), 0, 16);
        asm volatile("s_waitcnt vmcnt(0)" ::: "memory");
    }
    __syncthreads();
    if (tid == 0) __hip_atomic_store((GAS u32*)(p.ws + WS_ODONE) + bid, tag, RLX_AGENT);
    STAMP(11);
    if (!is_mlp) return;

    LAS u32* const top = (LAS u32*)(smem + MLP_TOP);
    u32x4 rb0, rb1;
    for (u32 spins = 0;; ++spins) {
        asm volatile("" ::: "memory");
        rb0 = __builtin_amdgcn_raw_buffer_load_b128(wsr, WS_RBUF + (u32)tid * 32u, 0, 16);
        rb1 = __builtin_amdgcn_raw_buffer_load_b128(wsr, WS_RBUF + (u32)tid * 32u + 16u, 0, 16);
        const bool ok = ((rb0.y | rb0.w | rb1.y | rb1.w) >> 16) == 0u;
        if (__all(ok) || spins > SPIN_CAP) break;
        __builtin_amdgcn_s_sleep(1);
    }
    STAMP(12);
    {
        u32 key[4];
        const u32 rr[4] = {rb0.x & 0xffffu, rb0.z & 0xffffu, rb1.x & 0xffffu, rb1.z & 0xffffu};
#pragma unroll
        for (int i = 0; i < 4; ++i) {
            u32 b = rr[i];
            if (b == 0x8000u) b = 0u;
            const u32 o = (b & 0x8000u) ? (~b & 0xffffu) : (b | 0x8000u);
            key[i] = (o << 16) | (u32)(2048 - (4 * tid + i));
        }
        u32 mine = 0;
#pragma unroll 1
        for (int it = 0; it < 8; ++it) {
            const u32 w = wave_umax(umax(umax(key[0], key[1]), umax(key[2], key[3])));
#pragma unroll
            for (int i = 0; i < 4; ++i) key[i] = key[i] == w ? 0u : key[i];
            mine = lane == it ? w : mine;
        }
        if (lane < 8) top[wave * 8 + lane] = mine;
    }
    __syncthreads();
    int tsel[8];
    {
        u32 k = top[lane];
#pragma unroll
        for (int it = 0; it < 8; ++it) {
            const u32 w = wave_umax(k);
            k = k == w ? 0u : k;
            tsel[it] = __builtin_amdgcn_readfirstlane(2048 - (int)(w & 0xffffu)) & 2047;
        }
    }
    STAMP(13);
    u64 rsel[8];
#pragma unroll
    for (int j = 0; j < 8; ++j) rsel[j] = __hip_atomic_load((GAS u64*)(p.ws + WS_RBUF) + tsel[j], RLX_AGENT);
    {
        const int t = wave == 0 ? tsel[0] : wave == 1 ? tsel[1] : wave == 2 ? tsel[2] : wave == 3 ? tsel[3]
                    : wave == 4 ? tsel[4] : wave == 5 ? tsel[5] : wave == 6 ? tsel[6] : tsel[7];
        const u64 rs = wave == 0 ? rsel[0] : wave == 1 ? rsel[1] : wave == 2 ? rsel[2] : wave == 3 ? rsel[3]
                     : wave == 4 ? rsel[4] : wave == 5 ? rsel[5] : wave == 6 ? rsel[6] : rsel[7];
        const int l56 = act56 ? lane : 0;
        u32 xv[8];
#pragma unroll
        for (int e = 0; e < 8; ++e)
            xv[e] = __hip_atomic_load((GAS u16*)(p.ws + WS_Z) + ((t >> 3) * D_ + 8 * l56 + e) * 8 + (t & 7), RLX_AGENT);
        const float m = bfhi((u32)rs), rd = __builtin_amdgcn_rcpf(bflo((u32)(rs >> 32)));
        u32x4 hp, xp;
#pragma unroll
        for (int i = 0; i < 4; ++i) {
            const f32x2 v = {bflo(xv[2 * i]), bflo(xv[2 * i + 1])};
            hp[i] = pkbf(rbf2(rbf2(rbf2(v - m) * rd) * cb1[i]) + sb[i]);
            xp[i] = xv[2 * i] | (xv[2 * i + 1] << 16);
        }
        if (act56) *(LAS u32x4*)(smem + MLP_H + wave * TRROW + lane * 16) = hp;
        if ((lane >> 1) == sid) *(LAS u32x4*)(smem + MLP_X1 + wave * 32 + (lane & 1) * 16) = xp;
        if (wave == 0 && (lane >> 1) == sid) {
#pragma unroll
            for (int i = 0; i < 4; ++i) *(LAS f32x2*)(smem + MLP_TOP + 512 + (lane & 1) * 32 + i * 8) = gb[i];
        }
    }
    __syncthreads();
    STAMP(14);
    {
        f32x4 acc = {0.f, 0.f, 0.f, 0.f};
        const LAS unsigned char* hb = smem + MLP_H + (r16 & 7) * TRROW + g4 * 16;
#pragma unroll
        for (int ks = 0; ks < 14; ++ks)
            acc = __builtin_amdgcn_mfma_f32_16x16x32_bf16(__builtin_bit_cast(bf16x8, wgu[ks]), *(const LAS bf16x8*)(hb + 64 * ks), acc, 0, 0, 0);
        u32x2 o;
#pragma unroll
        for (int k = 0; k < 2; ++k) {
            const u32 mine = pkbf(f32x2{acc[2 * k], acc[2 * k + 1]});
            const auto s = __builtin_amdgcn_permlane32_swap(mine, mine, false, false);
            const f32x2 gv = unbf(s[0]), uv = unbf(s[1]);
            const f32x2 sg = rbf2(f32x2{gv.x * sigmoidf_(gv.x), gv.y * sigmoidf_(gv.y)});
            o[k] = pkbf(sg * uv);
        }
        if (lane < 32 && r16 < 8)
            __hip_atomic_store((GAS u64*)(p.ws + WS_ACT + r16 * (2 * F_) + (64 * sid + 8 * wave + 4 * g4) * 2),
                               ((u64)o.y << 32) | o.x, RLX_AGENT);
    }
    STAMP(15);
    if (wave == 0) {
        const int tj = lane == 0 ? tsel[0] : lane == 1 ? tsel[1] : lane == 2 ? tsel[2] : lane == 3 ? tsel[3]
                     : lane == 4 ? tsel[4] : lane == 5 ? tsel[5] : lane == 6 ? tsel[6] : tsel[7];
        for (u32 spins = 0;; ++spins) {
            asm volatile("" ::: "memory");
            const u32 f = __hip_atomic_load((GAS u32*)(p.ws + WS_ODONE) + (tj >> 3), RLX_AGENT);
            if (__all(f == tag) || spins > SPIN_CAP) break;
            __builtin_amdgcn_s_sleep(1);
        }
    }
    {
        u32x4 av[7];
        for (u32 spins = 0;; ++spins) {
            asm volatile("" ::: "memory");
            bool ok = true;
#pragma unroll
            for (int ks = 0; ks < 7; ++ks)
                av[ks] = __builtin_amdgcn_raw_buffer_load_b128(wsr, WS_ACT + (u32)((r16 & 7) * (2 * F_) + (224 * wave + 32 * ks + 8 * g4) * 2), 0, 16);
#pragma unroll
            for (int ks = 0; ks < 7; ++ks) ok = ok && av[ks].x != ~0u && av[ks].y != ~0u && av[ks].z != ~0u && av[ks].w != ~0u;
            if (__all(ok) || spins > SPIN_CAP) break;
            __builtin_amdgcn_s_sleep(1);
        }
        STAMP(16);
        f32x4 acc = {0.f, 0.f, 0.f, 0.f};
#pragma unroll
        for (int ks = 0; ks < 7; ++ks)
            acc = __builtin_amdgcn_mfma_f32_16x16x32_bf16(__builtin_bit_cast(bf16x8, wdn[ks]), __builtin_bit_cast(bf16x8, av[ks]), acc, 0, 0, 0);
        if (r16 < 8) *(LAS f32x4*)(smem + MLP_RED + wave * 512 + r16 * 64 + g4 * 16) = acc;
    }
    __syncthreads();
    if (tid < 128) {
        const int j = tid >> 4, cc = tid & 15;
        float s = 0.f;
#pragma unroll
        for (int w = 0; w < 8; ++w) s += *(const LAS float*)(smem + MLP_RED + w * 512 + tid * 4);
        const int t = j == 0 ? tsel[0] : j == 1 ? tsel[1] : j == 2 ? tsel[2] : j == 3 ? tsel[3]
                    : j == 4 ? tsel[4] : j == 5 ? tsel[5] : j == 6 ? tsel[6] : tsel[7];
        const u32 rj = (u32)(j == 0 ? rsel[0] : j == 1 ? rsel[1] : j == 2 ? rsel[2] : j == 3 ? rsel[3]
                           : j == 4 ? rsel[4] : j == 5 ? rsel[5] : j == 6 ? rsel[6] : rsel[7]);
        const float rsg = rbf(sigmoidf_(bflo(rj)));
        const float y = rbf(rbf(s) * rsg);
        const float h2 = bflo(*(const LAS u16*)(smem + MLP_H + j * TRROW + (16 * sid + cc) * 2));
        const float x1 = bflo(*(const LAS u16*)(smem + MLP_X1 + j * 32 + cc * 2));
        const float gbv = *(const LAS float*)(smem + MLP_TOP + 512 + cc * 4);
        const float o = x1 + rbf(rbf(h2 + y) * gbv);
        __hip_atomic_store((GAS u16*)p.out + t * D_ + 16 * sid + cc, (u16)(pkbf(f32x2{o, 0.f}) & 0xffffu), RLX_AGENT);
    }
    STAMP(17);
}

extern "C" void kernel_launch(void* const* d_in, const int* in_sizes, int n_in, void* d_out, int out_size, void* d_ws,
                              size_t ws_size, hipStream_t stream) {
    Params p;
    p.x = (const u16*)d_in[0];
    p.a_re = (const u16*)d_in[1]; p.a_im = (const u16*)d_in[2];
    p.b_re = (const u16*)d_in[3]; p.b_im = (const u16*)d_in[4];
    p.c_re = (const u16*)d_in[5]; p.c_im = (const u16*)d_in[6];
    p.log_dt = (const u16*)d_in[7]; p.dskip = (const u16*)d_in[8];
    p.w_r = (const u16*)d_in[10]; p.w_gate = (const u16*)d_in[11]; p.w_up = (const u16*)d_in[12]; p.w_down = (const u16*)d_in[13];
    p.c = (const u16*)d_in[14]; p.w_mod = (const u16*)d_in[15];
    p.out = (u16*)d_out;
    p.ws = (unsigned char*)d_ws;
    fused_block<<<NWG, NT, 0, stream>>>(p);
}
```

```cpp
#include <hip/hip_runtime.h>
#include <stdint.h>

typedef unsigned int u32;
typedef unsigned long long u64;
typedef unsigned short u16;
typedef float f32x2 __attribute__((ext_vector_type(2)));
typedef float f32x4 __attribute__((ext_vector_type(4)));
typedef _Float16 f16x2 __attribute__((ext_vector_type(2)));
typedef _Float16 f16x8 __attribute__((ext_vector_type(8)));
typedef __bf16 bf16x2 __attribute__((ext_vector_type(2)));
typedef __bf16 bf16x8 __attribute__((ext_vector_type(8)));
typedef u32 u32x2 __attribute__((ext_vector_type(2)));
typedef short s16x4 __attribute__((ext_vector_type(4)));
typedef u32 u32x4 __attribute__((ext_vector_type(4)));

#define GAS __attribute__((address_space(1)))
#define LAS __attribute__((address_space(3)))
#define RLX_AGENT __ATOMIC_RELAXED, __HIP_MEMORY_SCOPE_AGENT
#define MEMFENCE() asm volatile("" ::: "memory")
#define SCHED() __builtin_amdgcn_sched_barrier(0)
#define LDWS(off) __builtin_amdgcn_raw_buffer_load_b128(wsr, (u32)(off), 0, 16)
#define STWS(v, off) __builtin_amdgcn_raw_buffer_store_b128(v, wsr, (u32)(off), 0, 16)

constexpr int L_ = 2048, D_ = 448, N_ = 256, F_ = 1792;
constexpr int NWG = 256, NT = 512;

constexpr u32 WS_EPOCH = 0;
constexpr u32 WS_MODB = 1024;
constexpr u32 WS_RBUF = WS_MODB + 8192;
constexpr u32 WS_ODONE = WS_RBUF + 8192;
constexpr u32 WS_PD = WS_ODONE + 2048;
constexpr u32 WS_MP = WS_ODONE + 4096;
constexpr u32 WS_PART = WS_MP + 784 * 512;
constexpr u32 WS_Z = WS_PART + 28 * 14336;
constexpr u32 WS_H2 = WS_Z + D_ * L_ * 2;
constexpr u32 WS_XT = WS_H2 + D_ * L_ * 2;
constexpr u32 WS_END = WS_XT + 256 * 14336;
constexpr u32 WS_DBG = 12u << 20;

constexpr int FROW = 1056;
constexpr int FCH = 53 * FROW;
constexpr int LDS_F = 0;
constexpr int KCOPY = 4160;
constexpr int LDS_KC4 = 0;
constexpr int LDS_XCH = 2 * 4 * KCOPY;
constexpr int LDS_KD = 116 * 1024;
constexpr int KDCH = 1312;
constexpr int LDS_HS = LDS_KD + 2 * KDCH;
constexpr int HSCH = 4608;
constexpr int TRROW = 912;
constexpr int LDS_TR = LDS_HS + 2 * HSCH;
constexpr int LDS_MB = LDS_TR + 8 * TRROW;
constexpr int LDS_MISC = LDS_MB + 4 * 896;
constexpr int LDS_HD = LDS_MISC + 1024;
constexpr int LDS_TOTAL = LDS_HD + 8192;
constexpr int MLP_H = LDS_KD;
constexpr int MLP_ACT = MLP_H + 8 * TRROW;
constexpr int MLP_TOP = MLP_ACT + 8 * 144;
constexpr int MLP_RED = 0;
static_assert(MLP_TOP + 1024 <= LDS_TR && LDS_KD + 2048 + 64 * 56 * 4 <= LDS_MB, "lds");
static_assert(2 * FCH <= LDS_KD && 2 * 57344 <= LDS_KD && LDS_XCH + 16384 <= LDS_KD, "lds");
static_assert(LDS_TOTAL <= 160 * 1024, "lds");
static_assert(LDS_TOTAL > 80 * 1024, "one workgroup per CU");

constexpr u32 SPIN_CAP = 1u << 14;

struct KTab { u16 v[2064]; };
constexpr KTab make_ktab() {
    KTab t{};
    for (int x = 0; x < 2064; ++x) {
        int l = 2047 - x, idx = 641;
        if (l >= 0) {
            int k = l < 256 ? 0 : l < 512 ? 1 : l < 1024 ? 2 : 3;
            int q = l >> k, rem = l & ((1 << k) - 1), half = (1 << k) >> 1;
            if (k > 0 && (rem > half || (rem == half && (q & 1)))) q += 1;
            idx = 128 * k + q;
        }
        t.v[x] = (u16)(2 * idx);
    }
    return t;
}
__device__ const KTab g_ktab __attribute__((aligned(16))) = make_ktab();

struct Params {
    const u16 *x, *a_re, *a_im, *b_re, *b_im, *c_re, *c_im, *log_dt, *dskip, *w_r, *w_gate, *w_up, *w_down, *c, *w_mod;
    u16* out;
    unsigned char* ws;
};

__device__ __forceinline__ float bflo(u32 u) { return __uint_as_float(u << 16); }
__device__ __forceinline__ float bfhi(u32 u) { return __uint_as_float(u & 0xffff0000u); }
__device__ __forceinline__ u32 pkbf(f32x2 a) { return __builtin_bit_cast(u32, __builtin_convertvector(a, bf16x2)); }
__device__ __forceinline__ u32 pkh(f32x2 a) { return __builtin_bit_cast(u32, __builtin_convertvector(a, f16x2)); }
__device__ __forceinline__ f32x2 unbf(u32 u) { return f32x2{bflo(u), bfhi(u)}; }
__device__ __forceinline__ f32x2 rbf2(f32x2 a) { return unbf(pkbf(a)); }
__device__ __forceinline__ float rbf(float a) { return bflo(pkbf(f32x2{a, 0.f})); }
__device__ __forceinline__ f32x2 pfma(f32x2 a, f32x2 b, f32x2 c) { return __builtin_elementwise_fma(a, b, c); }
__device__ __forceinline__ f32x2 cmul(f32x2 a, f32x2 b, f32x2 brot) {
    return pfma(a.yy, brot, a.xx * b);
}
template <int CTRL> __device__ __forceinline__ float dppf(float x) {
    return __builtin_bit_cast(float, __builtin_amdgcn_mov_dpp(__builtin_bit_cast(int, x), CTRL, 0xf, 0xf, true));
}
template <int CTRL> __device__ __forceinline__ u32 dppu(u32 x) {
    return (u32)__builtin_amdgcn_mov_dpp((int)x, CTRL, 0xf, 0xf, true);
}
constexpr int XOR1 = 0xB1, XOR2 = 0x4E, HMIR = 0x141, MIR = 0x140, ROR8 = 0x128;
__device__ __forceinline__ float swap16_add(float a, float b) {
    auto s = __builtin_amdgcn_permlane16_swap(__float_as_uint(a), __float_as_uint(b), false, false);
    return __uint_as_float(s[0]) + __uint_as_float(s[1]);
}
__device__ __forceinline__ float swap32_add(float a, float b) {
    auto s = __builtin_amdgcn_permlane32_swap(__float_as_uint(a), __float_as_uint(b), false, false);
    return __uint_as_float(s[0]) + __uint_as_float(s[1]);
}
__device__ __forceinline__ float row_sum(float x) {
    x += dppf<XOR1>(x); x += dppf<XOR2>(x); x += dppf<HMIR>(x); x += dppf<MIR>(x);
    return x;
}
__device__ __forceinline__ float wave_sum(float x) {
    x = row_sum(x);
    x = swap16_add(x, x);
    return swap32_add(x, x);
}
__device__ __forceinline__ float rdl(float x, int l) { return __uint_as_float(__builtin_amdgcn_readlane(__float_as_uint(x), l)); }
__device__ __forceinline__ void wave_sum4(float& a, float& b, float& c, float& d) {
    const float x = row_sum(swap16_add(swap32_add(a, c), swap32_add(b, d)));
    a = rdl(x, 0); b = rdl(x, 16); c = rdl(x, 32); d = rdl(x, 48);
}
__device__ __forceinline__ void wave_sum2(float& a, float& b) {
    const float y = swap32_add(a, b);
    const float x = row_sum(swap16_add(y, y));
    a = rdl(x, 0); b = rdl(x, 32);
}
__device__ __forceinline__ u32 umax(u32 a, u32 b) { return a > b ? a : b; }
__device__ __forceinline__ u32 umin(u32 a, u32 b) { return a < b ? a : b; }
__device__ __forceinline__ u32 cls_umax(u32 x) {
    x = umax(x, dppu<ROR8>(x));
    auto s = __builtin_amdgcn_permlane16_swap(x, x, false, false);
    x = umax(s[0], s[1]);
    auto t = __builtin_amdgcn_permlane32_swap(x, x, false, false);
    return umax(t[0], t[1]);
}
__device__ __forceinline__ u32 wave_umax(u32 x) {
    x = umax(x, dppu<XOR1>(x)); x = umax(x, dppu<XOR2>(x)); x = umax(x, dppu<HMIR>(x));
    return cls_umax(x);
}
__device__ __forceinline__ u32 mbcnt64(u64 m) {
    return __builtin_amdgcn_mbcnt_hi((u32)(m >> 32), __builtin_amdgcn_mbcnt_lo((u32)m, 0u));
}
__device__ __forceinline__ u32 umax4(u32x4 v) { return umax(umax(v.x, v.y), umax(v.z, v.w)); }
__device__ __forceinline__ float pow2i(int e) {
    e = e > 127 ? 127 : e;
    return e < -126 ? 0.f : __uint_as_float((u32)(e + 127) << 23);
}
__device__ __forceinline__ float pow2c(int e) {
    e = e > 127 ? 127 : e < -126 ? -126 : e;
    return __uint_as_float((u32)(e + 127) << 23);
}
__device__ __forceinline__ float sigmoidf_(float x) { return __builtin_amdgcn_rcpf(1.f + __expf(-x)); }
__device__ __forceinline__ void ln_finish(float pv, float s1, float s2, float& m, float& rd) {
    const float mu = s1 * (1.f / D_);
    const float var = fmaxf(fmaf(-mu, mu, s2 * (1.f / D_)), 0.f);
    m = rbf(pv + mu);
    rd = __builtin_amdgcn_rcpf(rbf(__builtin_amdgcn_sqrtf(rbf(rbf(var) + 1e-5f))));
}

__device__ __forceinline__ void sub_arrive(LAS u32* cnt, int lane) {
    MEMFENCE();
    if (lane == 0) __hip_atomic_fetch_add(cnt, 1u, __ATOMIC_RELAXED, __HIP_MEMORY_SCOPE_WORKGROUP);
}
__device__ __forceinline__ void sub_wait(LAS u32* cnt, u32 n) {
    while ((u32)__builtin_amdgcn_readfirstlane(*(volatile LAS u32*)cnt) < n) { }
    MEMFENCE();
}
__device__ __forceinline__ float exp_acc(float x) {
    const float t = x * 1.44269504088896341f;
    const float e = fmaf(x, 1.9259630e-08f, fmaf(x, 1.44269504088896341f, -t));
    const float r = __builtin_amdgcn_exp2f(t);
    return fmaf(r, e * 0.69314718056f, r);
}

#define STAMP(i) do { } while (0)

template <int R0, int R1, int R2, int R3>
__device__ __forceinline__ void put4(u32 m0v, u32 a, u32 b, u32 c, u32 d) {
    asm volatile("s_mov_b32 m0, %4\n\ts_nop 0\n\t"
                 "ds_write_addtid_b32 %0 offset:%5\n\tds_write_addtid_b32 %1 offset:%6\n\t"
                 "ds_write_addtid_b32 %2 offset:%7\n\tds_write_addtid_b32 %3 offset:%8"
                 :: "v"(a), "v"(b), "v"(c), "v"(d), "s"(m0v), "i"(R0 * FROW), "i"(R1 * FROW), "i"(R2 * FROW), "i"(R3 * FROW)
                 : "memory");
}

template <int VH>
__device__ __forceinline__ void conv_all(const LAS unsigned char* pa, const LAS unsigned char* pa2, const LAS unsigned char* pb,
                                         f32x4 (&acc)[8]) {
    u32x2 Alo[2][8], Ahi[2][8];
    f16x8 B[2][8];
#pragma unroll
    for (int i = 0; i < 8; ++i) {
        Alo[0][i] = *(const LAS u32x2*)(pa + 192 + 512 * (7 - i));
        Ahi[0][i] = *(const LAS u32x2*)(pa2 + 192 + 512 * (7 - i));
        B[0][i] = *(const LAS f16x8*)(pb + 192 + 512 * i);
        SCHED();
    }
#pragma unroll
    for (int vi = 0; vi < 4; ++vi) {
        const int cur = vi & 1, nxt = cur ^ 1;
        int n = 0;
#pragma unroll
        for (int s = 0; s < 8; ++s) {
#pragma unroll
            for (int a = 0; a < 8; ++a) {
#pragma unroll
                for (int b = 0; b < 8; ++b) {
                    if (a + b < 8 && (a > b ? a : b) == s) {
                        const int k = (a + b) ^ (4 * VH);
                        const f16x8 Af = __builtin_bit_cast(f16x8, u32x4{Alo[cur][b].x, Alo[cur][b].y, Ahi[cur][b].x, Ahi[cur][b].y});
                        acc[k] = __builtin_amdgcn_mfma_f32_16x16x32_f16(Af, B[cur][a], acc[k], 0, 0, 0);
                        if (vi < 3 && n >= 4 && n % 3 == 1 && (n - 4) / 3 < 8) {
                            const int i = (n - 4) / 3, off = 192 - 64 * (vi + 1);
                            Alo[nxt][i] = *(const LAS u32x2*)(pa + off + 512 * (7 - i));
                            Ahi[nxt][i] = *(const LAS u32x2*)(pa2 + off + 512 * (7 - i));
                            B[nxt][i] = *(const LAS f16x8*)(pb + off + 512 * i);
                        }
                        SCHED();
                        ++n;
                    }
                }
            }
        }
    }
}

__global__ void __launch_bounds__(NT, 2) fused_block(Params p) {
    __shared__ __attribute__((aligned(16))) unsigned char smem_[LDS_TOTAL];
    LAS unsigned char* const smem = (LAS unsigned char*)smem_;
    const int tid = threadIdx.x, lane = tid & 63;
    const int wave = __builtin_amdgcn_readfirstlane(tid >> 6);
    const int bid = blockIdx.x;
    const int xg = bid & 7, xi = bid >> 3;
    const bool is_conv = xi < 28;
    const int pair = xg * 28 + xi, d0 = 2 * pair;
    const int sid = xg * 4 + (xi - 28);
    const bool is_mlp = !is_conv && sid < 28;
    const int tg = xg * 32 + xi;
    const int town = 8 * tg + wave;
    const __amdgpu_buffer_rsrc_t wsr = __builtin_amdgcn_make_buffer_rsrc(p.ws, 0, WS_END, 0x00020000);
    const int r16 = lane & 15, g4 = lane >> 4;
    const bool act56 = lane < 56;
    const int l56 = act56 ? lane : 55;
    const int t448 = tid < D_ ? tid : D_ - 1;
    const u32x4 ones = {~0u, ~0u, ~0u, ~0u};
    LAS float* const misc = (LAS float*)(smem + LDS_MISC);
    STAMP(0);

    u32x4 xrow = *(const GAS u32x4*)(p.x + town * D_ + l56 * 8);
    u32 tag = __hip_atomic_load((GAS u32*)(p.ws + WS_EPOCH), RLX_AGENT);
    MEMFENCE();
    u32x4 xtp[2];
    auto ln1_compute = [&]() {
        asm volatile("" : "+v"(xrow), "+v"(tag));
        tag = __builtin_amdgcn_readfirstlane(tag) + 1u;
        STAMP(20);
        const f32x2 v[4] = {unbf(xrow.x), unbf(xrow.y), unbf(xrow.z), unbf(xrow.w)};
        const float pv = __uint_as_float(__builtin_amdgcn_readfirstlane(xrow.x << 16));
        f32x2 s1 = {0.f, 0.f}, s2 = {0.f, 0.f};
#pragma unroll
        for (int i = 0; i < 4; ++i) { const f32x2 dlt = v[i] - pv; s1 += dlt; s2 = pfma(dlt, dlt, s2); }
        float a = act56 ? s1.x + s1.y : 0.f, b = act56 ? s2.x + s2.y : 0.f;
        wave_sum2(a, b);
        float m, rd;
        ln_finish(pv, a, b, m, rd);
        u32x4 xn;
#pragma unroll
        for (int i = 0; i < 4; ++i) xn[i] = pkbf(rbf2(v[i] - m) * rd);
        *(LAS u32x4*)(smem + LDS_TR + wave * TRROW + l56 * 16) = xn;
        const LAS unsigned char* src = smem + LDS_TR + (tid & 1) * 4 * TRROW + (tid >> 1) * 2;
        __syncthreads();
        STAMP(21);
#pragma unroll
        for (int k = 0; k < 2; ++k) {
            const u32 t0 = *(const LAS u16*)(src + 512 * k), t1 = *(const LAS u16*)(src + 512 * k + TRROW);
            const u32 t2 = *(const LAS u16*)(src + 512 * k + 2 * TRROW), t3 = *(const LAS u16*)(src + 512 * k + 3 * TRROW);
            xtp[k] = u32x4{t0 | (t1 << 16), tag, t2 | (t3 << 16), tag};
        }
    };
    auto ln1_store = [&]() {
        const u32 off = WS_XT + (u32)(tg * 14336 + tid * 16);
        STWS(xtp[0], off);
        STWS(xtp[1], tid < 384 ? off + 8192u : WS_END);
    };

    auto fetch_modb = [&]() {
        const int t = tid - 256, t2 = min(t + 256, D_ - 1);
        u32x4 ma, mb;
        for (u32 spins = 0;; ++spins) {
            MEMFENCE();
            ma = LDWS(WS_MODB + t * 16); mb = LDWS(WS_MODB + t2 * 16);
            if (__all(ma.y == tag && ma.w == tag && mb.y == tag && mb.w == tag) || spins > SPIN_CAP) break;
            __builtin_amdgcn_s_sleep(16);
        }
        *(LAS u32x2*)(smem + LDS_MB + t * 8) = u32x2{ma.x, ma.z};
        *(LAS u32x2*)(smem + LDS_MB + t2 * 8) = u32x2{mb.x, mb.z};
    };
    auto check_pd = [&]() {
        for (u32 spins = 0;; ++spins) {
            MEMFENCE();
            const u32x4 pd = LDWS(WS_PD + lane * 16);
            if (__all(pd.x == tag && pd.y == tag && pd.z == tag && pd.w == tag) || spins > SPIN_CAP) break;
            __builtin_amdgcn_s_sleep(4);
        }
    };

    u32x4 wgu[14];
    u32x4 wdn[8];

    if (is_conv) {
        const int ch = wave >> 2;
        LAS u32* const cnt = (LAS u32*)(misc + 28);
        const int o = d0 * N_ + tid;
        u32 q_are = p.a_re[o], q_aim = p.a_im[o], q_bre = p.b_re[o], q_bim = p.b_im[o], q_cre = p.c_re[o], q_cim = p.c_im[o];
        u32 q_ldt = p.log_dt[d0 + ch];

        auto gen_tables = [&](auto hook) {
            const float dt = rbf(exp_acc(bflo(q_ldt)));
            float lr = dt * bflo(q_are);
            const float li = dt * bflo(q_aim);
            const float bre = dt * bflo(q_bre), bim = dt * bflo(q_bim);
            const float cre = bflo(q_cre), cim = bflo(q_cim);
            const float cr = cre * bre - cim * bim, ci = cre * bim + cim * bre;
            const float mw = __uint_as_float(wave_umax(umax(__float_as_uint(cr) & 0x7fffffffu, __float_as_uint(ci) & 0x7fffffffu)));
            const int ew = __builtin_amdgcn_frexp_expf(mw);
            float gw = 0.f;
            if (__any(lr > 0.f)) {
                gw = __uint_as_float(wave_umax(__float_as_uint(fmaxf(lr, 0.f))));
                lr -= gw;
            }
            if (lane == 0) { ((LAS int*)misc)[wave] = ew; misc[8 + wave] = gw; }
            const float a2 = lr * 1.44269504088896341f;
            const float ph = li * 0.15915494309189535f;
            const float pe = fmaf(li, 6.4206383e-09f, fmaf(li, 0.15915494309189535f, -ph));
            auto anchor = [&](float m) {
                const float mag = __builtin_amdgcn_exp2f(a2 * m);
                const float rev = fmaf(pe, m, __builtin_amdgcn_fractf(ph * m));
                return f32x2{__builtin_amdgcn_cosf(rev), __builtin_amdgcn_sinf(rev)} * mag;
            };
            const f32x2 e1 = anchor(1.f), e256 = anchor(256.f);
            f32x2 e8 = anchor(8.f), e64 = anchor(64.f);
            e8.y = -e8.y; e64.y = -e64.y;
            const f32x2 e1r = {-e1.y, e1.x}, e256r = {-e256.y, e256.x}, e8r = {-e8.y, e8.x}, e64r = {-e64.y, e64.x};
            const u32 m0v = (u32)(uintptr_t)(smem + LDS_F) + (u32)(ch * FCH + (wave & 3) * 256);
            f32x2 b0 = f32x2{__builtin_amdgcn_ldexpf(cr, 14 - ew), -__builtin_amdgcn_ldexpf(ci, 14 - ew)};
            const f32x2 s1 = e1 * 32768.f;
            f32x2 u0 = e256 * 32768.f;
            f32x2 b1 = cmul(b0, e64, e64r);
            const f32x2 s2 = cmul(s1, e1, e1r);
            const f32x2 v0 = cmul(u0, e256, e256r);
            f32x2 b2 = cmul(b1, e64, e64r);
            const f32x2 s3 = cmul(s2, e1, e1r);
            const f32x2 w0 = cmul(v0, e256, e256r);
            f32x2 b3 = cmul(b2, e64, e64r);
            const f32x2 s4 = cmul(s3, e1, e1r);
            put4<0, 1, 2, 3>(m0v, 0x00007800u, pkh(s1), pkh(s2), pkh(s3));
            put4<8, 12, 14, 4>(m0v, pkh(u0), pkh(v0), pkh(w0), pkh(s4));
            u0 = cmul(w0, e256, e256r);
            const f32x2 s5 = cmul(s4, e1, e1r);
            const f32x2 u2 = cmul(s2, e256, e256r), u4 = cmul(s4, e256, e256r);
            put4<16, 5, 9, 10>(m0v, pkh(u0), pkh(s5), pkh(u2), pkh(u4));
            u0 = cmul(u0, e256, e256r);
            const f32x2 s6 = cmul(s5, e1, e1r);
            const f32x2 v4 = cmul(u4, e256, e256r);
            const u32 k1280 = pkh(u0);
            u0 = cmul(u0, e256, e256r);
            put4<17, 6, 13, 18>(m0v, k1280, pkh(s6), pkh(v4), pkh(u0));
            u0 = cmul(u0, e256, e256r);
            const f32x2 s7 = cmul(s6, e1, e1r);
            const f32x2 u6 = cmul(s6, e256, e256r), w4 = cmul(v4, e256, e256r);
            put4<19, 7, 11, 15>(m0v, pkh(u0), pkh(s7), pkh(u6), pkh(w4));
            u0 = cmul(u0, e256, e256r);
            asm volatile("s_mov_b32 m0, %1\n\ts_nop 0\n\tds_write_addtid_b32 %0 offset:%2" :: "v"(pkh(u0)), "s"(m0v), "i"(20 * FROW) : "memory");
#define BSTEP(j) put4<21 + j, 29 + j, 37 + j, 45 + j>(m0v, pkh(b0), pkh(b1), pkh(b2), pkh(b3)); \
            if (j < 7) { b0 = cmul(b0, e8, e8r); b1 = cmul(b1, e8, e8r); b2 = cmul(b2, e8, e8r); b3 = cmul(b3, e8, e8r); }
            BSTEP(0) BSTEP(1) BSTEP(2)
            hook();
            BSTEP(3) BSTEP(4) BSTEP(5) BSTEP(6) BSTEP(7)
#undef BSTEP
            asm volatile("s_waitcnt lgkmcnt(0)" ::: "memory");
        };

        if (wave < 4) {
            u32x4 kt0 = *(const u32x4*)(g_ktab.v + 8 * tid);
            u32x2 kt1 = *(const u32x2*)(g_ktab.v + 8 * tid + 8);
            const u32 pf0 = *(const GAS u32*)(p.w_mod + (16 * xi + (lane >> 2)) * (6 * D_) + 2 * D_ + 224 * xg + (wave & 1) * 30 + (lane & 3) * 64);
            MEMFENCE();
            if (tid < 64) *(LAS u32x4*)(smem + LDS_HS + (tid >> 5) * HSCH + (tid & 31) * 16) = u32x4{0u, 0u, 0u, 0u};
            if (tid >= 64 && tid < 66) *(LAS u16*)(smem + LDS_KD + (tid - 64) * KDCH + 2 * 641) = 0;
            if (tid >= 66 && tid < 68) *(LAS u32x4*)(cnt + 4 * (tid - 66)) = u32x4{0u, 0u, 0u, 0u};
            const int xw = min(xg, 6);
            const u16* pfp = (wave == 0 ? p.w_gate + 16 * xi * F_ + 256 * xw : wave == 1 ? p.w_up + 16 * xi * F_ + 256 * xw
                                                                                        : p.w_down + 256 * xw * D_ + 4096 * xi)
                             + (lane >> 2) * (wave < 2 ? F_ : 256) + (lane & 3) * 64;
            SCHED();
            ln1_compute();
            asm volatile("" : "+v"(q_are), "+v"(q_aim), "+v"(q_bre), "+v"(q_bim), "+v"(q_cre), "+v"(q_cim), "+v"(q_ldt), "+v"(kt0), "+v"(kt1));
            const u32 pf1 = *(const GAS u32*)pfp;
            ln1_store();
            SCHED();
            STAMP(1);
            gen_tables([] {});
            __syncthreads();
            STAMP(2);

            const int kc = wave >> 1, ta = wave & 1;
            int kexp;
            {
                const LAS unsigned char* pa = smem + LDS_F + kc * FCH + (16 * ta + r16) * FROW + g4 * 16;
                const LAS unsigned char* pb = smem + LDS_F + kc * FCH + (21 + r16) * FROW + g4 * 16;
                const u32x4 ewv = *(const LAS u32x4*)(misc + 4 * kc);
                const f32x4 gwv = *(const LAS f32x4*)(misc + 8 + 4 * kc);
                f16x8 fa[16], fb0[16], fb1[16];
#pragma unroll
                for (int ks = 0; ks < 8; ++ks) {
                    fa[ks] = *(const LAS f16x8*)(pa + 64 * ks); fb0[ks] = *(const LAS f16x8*)(pb + 64 * ks);
                    fb1[ks] = *(const LAS f16x8*)(pb + 16 * FROW + 64 * ks);
                }
                SCHED();
                const int cs = g4 < 2 ? 8 : g4 == 2 ? 4 : 2;
                const int ib0 = (g4 < 2 ? 4 * g4 : g4 == 2 ? 256 : 384) + cs * r16;
                const bool v1 = g4 == 0, v20 = lane == 16;
                const int ibase = ta ? (v1 ? 512 + r16 : v20 ? 640 : 644) : ib0;
                const int o1 = ta ? (v1 ? 32 : v20 ? 4 : 0) : 1;
                const int o2 = ta ? (v1 ? 64 : v20 ? 5 : 0) : (g4 == 3 ? 64 : 2);
                const int ob = ta ? (v1 ? 16 : v20 ? 4 : 0) : 16 * cs;
                int ews[4];
#pragma unroll
                for (int w4 = 0; w4 < 4; ++w4) { const u32 e = ewv[w4]; ews[w4] = __builtin_amdgcn_readfirstlane((int)e); }
                const int emax = max(max(ews[0], ews[1]), max(ews[2], ews[3]));
                const float sc[4] = {pow2i(ews[0] - emax - 23), pow2i(ews[1] - emax - 23), pow2i(ews[2] - emax - 23), pow2i(ews[3] - emax - 23)};
                const u32 gany = __builtin_amdgcn_readfirstlane(__float_as_uint(gwv.x) | __float_as_uint(gwv.y) | __float_as_uint(gwv.z) | __float_as_uint(gwv.w));
                LAS _Float16* kd = (LAS _Float16*)(smem + LDS_KD + kc * KDCH) + ibase;
                SCHED();
                f32x4 acc[2][4];
#pragma unroll
                for (int ks = 0; ks < 16; ++ks) {
                    const f32x4 zero = {0.f, 0.f, 0.f, 0.f};
                    acc[0][ks >> 2] = __builtin_amdgcn_mfma_f32_16x16x32_f16(fa[ks], fb0[ks], (ks & 3) ? acc[0][ks >> 2] : zero, 0, 0, 0);
                    acc[1][ks >> 2] = __builtin_amdgcn_mfma_f32_16x16x32_f16(fa[ks], fb1[ks], (ks & 3) ? acc[1][ks >> 2] : zero, 0, 0, 0);
                    if (ks < 8) {
                        fa[ks + 8] = *(const LAS f16x8*)(pa + 64 * (ks + 8)); fb0[ks + 8] = *(const LAS f16x8*)(pb + 64 * (ks + 8));
                        fb1[ks + 8] = *(const LAS f16x8*)(pb + 16 * FROW + 64 * (ks + 8));
                    }
                    SCHED();
                }
                kexp = emax - 6;
                float G = 0.f;
                if (gany) {
                    const float gmax = fmaxf(fmaxf(gwv[0], gwv[1]), fmaxf(gwv[2], gwv[3]));
                    G = fminf(ceilf(gmax * (2048.f * 1.44269504088896341f)), 1000.f);
                    kexp += __builtin_amdgcn_readfirstlane((int)G);
                }
#pragma unroll
                for (int tb = 0; tb < 2; ++tb) {
                    f32x4 val;
                    if (gany) {
                        const f32x4 g2 = gwv * 1.44269504088896341f;
#pragma unroll
                        for (int reg = 0; reg < 4; ++reg) {
                            const int i = min(ibase + tb * ob + (reg & 1) * o1 + (reg >> 1) * o2, 640);
                            const int k = i < 256 ? 0 : i < 384 ? 1 : i < 512 ? 2 : 3;
                            const float lp = (float)((i - 128 * k) << k);
                            val[reg] = acc[tb][0][reg] * sc[0] * __builtin_amdgcn_exp2f(fmaf(g2.x, lp, -G)) + acc[tb][1][reg] * sc[1] * __builtin_amdgcn_exp2f(fmaf(g2.y, lp, -G))
                                     + acc[tb][2][reg] * sc[2] * __builtin_amdgcn_exp2f(fmaf(g2.z, lp, -G)) + acc[tb][3][reg] * sc[3] * __builtin_amdgcn_exp2f(fmaf(g2.w, lp, -G));
                        }
                    } else {
                        val = acc[tb][0] * sc[0];
                        val = __builtin_elementwise_fma(acc[tb][1], (f32x4)sc[1], val);
                        val = __builtin_elementwise_fma(acc[tb][2], (f32x4)sc[2], val);
                        val = __builtin_elementwise_fma(acc[tb][3], (f32x4)sc[3], val);
                    }
                    LAS _Float16* kq = kd + tb * ob;
                    kq[0] = (_Float16)val[0]; kq[o1] = (_Float16)val[1]; kq[o2] = (_Float16)val[2]; kq[o1 + o2] = (_Float16)val[3];
                }
            }
            sub_arrive(cnt, lane);
            const u32 tb[6] = {kt0.x, kt0.y, kt0.z, kt0.w, kt1.x, kt1.y};
            u32 tlo[6], thi[6];
#pragma unroll
            for (int i = 0; i < 6; ++i) { tlo[i] = tb[i] & 0xffffu; thi[i] = tb[i] >> 16; }
            sub_wait(cnt, 4);
            STAMP(3);

#pragma unroll
            for (int c2 = 0; c2 < 2; ++c2) {
                const LAS unsigned char* kd = smem + LDS_KD + c2 * KDCH;
                u32 rg[6];
#pragma unroll
                for (int i = 0; i < 6; ++i) {
                    const u32 lo = *(const LAS u16*)(kd + tlo[i]);
                    const u32 hi = *(const LAS u16*)(kd + thi[i]);
                    rg[i] = lo | (hi << 16);
                }
                u32 al[5];
#pragma unroll
                for (int i = 0; i < 5; ++i) al[i] = __builtin_amdgcn_alignbit(rg[i + 1], rg[i], 16);
                LAS unsigned char* kcp = smem + LDS_KC4 + c2 * 4 * KCOPY + tid * 16;
                *(LAS u32x4*)(kcp) = u32x4{rg[0], rg[1], rg[2], rg[3]};
                *(LAS u32x4*)(kcp + KCOPY) = u32x4{al[0], al[1], al[2], al[3]};
                *(LAS u32x4*)(kcp + 2 * KCOPY) = u32x4{rg[1], rg[2], rg[3], rg[4]};
                *(LAS u32x4*)(kcp + 3 * KCOPY) = u32x4{al[1], al[2], al[3], al[4]};
            }
            if (tid < 16) *(LAS u32x4*)(smem + LDS_KC4 + (tid >> 1) * KCOPY + 4096 + (tid & 1) * 16) = u32x4{0u, 0u, 0u, 0u};
            asm volatile("" :: "v"(pf0), "v"(pf1));
            const int vh = ta;
            const LAS unsigned char* pa = smem + LDS_KC4 + (kc * 4 + 3 - (r16 & 3)) * KCOPY
                                          + 2 * (2044 - 256 * 7 - 16 * (8 * vh + 7) - 4 * (r16 >> 2) + 8 * g4);
            const LAS unsigned char* pb = smem + LDS_HS + kc * HSCH + 512 - 32 * (8 * vh + 7) + 32 * r16 + 16 * g4;
            const LAS unsigned char* pa2 = pa + 8;
            asm volatile("" : "+v"(pa2));
            const u32 z_off = WS_Z + (u32)((d0 + kc) * (2 * L_) + 2048 * vh + 512 * (g4 & 1) + 32 * r16 + 16 * (g4 >> 1));
            STAMP(4);
            __syncthreads();
            STAMP(6);

            f32x4 acc[8];
#pragma unroll
            for (int k = 0; k < 8; ++k) acc[k] = f32x4{0.f, 0.f, 0.f, 0.f};
            if (vh) conv_all<1>(pa, pa2, pb, acc); else conv_all<0>(pa, pa2, pb, acc);
            STAMP(7);
            LAS f32x4* xw4 = (LAS f32x4*)(smem + LDS_XCH + wave * 4096) + lane;
#pragma unroll
            for (int k = 0; k < 4; ++k) xw4[64 * k] = acc[4 + k];
            sub_arrive(cnt + 4 + kc, lane);
            sub_arrive(cnt + 3, lane);
            u32x2 hdv[4];
#pragma unroll
            for (int k = 0; k < 4; ++k) hdv[k] = *(const LAS u32x2*)(smem + LDS_HD + kc * 4096 + 2048 * vh + 512 * k + 32 * r16 + 8 * g4);
            const int ye = kexp + __builtin_amdgcn_readfirstlane(((LAS int*)misc)[24 + kc]);
            const float ys0 = pow2c(ye), ys1 = pow2c(ye - max(min(ye, 127), -126));
            sub_wait(cnt + 2, 4);
            sub_wait(cnt + 4 + kc, 2);
            const LAS f32x4* xr = (const LAS f32x4*)(smem + LDS_XCH + (wave ^ 1) * 4096) + lane;
            const bool two = __float_as_uint(ys1) != 0x3f800000u;
#pragma unroll
            for (int k2 = 0; k2 < 2; ++k2) {
                f32x4 ya = (xr[128 * k2] + acc[2 * k2]) * ys0, yb = (xr[128 * k2 + 64] + acc[2 * k2 + 1]) * ys0;
                if (two) { ya *= ys1; yb *= ys1; }
                const u32 a0 = pkbf(rbf2(ya.xy) + unbf(hdv[2 * k2].x)), a1 = pkbf(rbf2(ya.zw) + unbf(hdv[2 * k2].y));
                const u32 b0 = pkbf(rbf2(yb.xy) + unbf(hdv[2 * k2 + 1].x)), b1 = pkbf(rbf2(yb.zw) + unbf(hdv[2 * k2 + 1].y));
                const auto s0 = __builtin_amdgcn_permlane16_swap(a0, b0, false, false);
                const auto s1 = __builtin_amdgcn_permlane16_swap(a1, b1, false, false);
                STWS((u32x4{s0[0], s1[0], s0[1], s1[1]}), z_off + 1024u * k2);
            }
            STAMP(19);
        } else {
            const int t4 = tid - 256;
            const int unit = min(pair * 4 + (wave & 3), 783), ucb = unit / 56, urow = (unit - ucb * 56) * 8 + (lane >> 3);
            u32 q_c = p.c[urow];
            u32x4 wmv = *(const GAS u32x4*)(p.w_mod + urow * (6 * D_) + ucb * 64 + (lane & 7) * 8);
            const int hch = (lane >> 1) & 1;
            u32 q_dsk = p.dskip[d0 + hch];
            MEMFENCE();
            const u32 xt_off = WS_XT + (u32)((t4 >> 2) * 14336 + d0 * 32 + (t4 & 3) * 16);
            const int mcol = (wave & 1) * D_ + d0;
            const u32 mp_off = wave < 6 ? WS_MP + (u32)(((mcol >> 6) * 56 + l56) * 512 + ((mcol & 63) >> 1) * 16) : WS_END;
            const u32 mpw_off = ((lane & 8) || pair * 4 + (wave & 3) >= 784) ? WS_END : WS_MP + (u32)(unit * 512 + ((lane & 7) * 4 + g4) * 16);
            SCHED();
            ln1_compute();
            asm volatile("" : "+v"(q_are), "+v"(q_aim), "+v"(q_bre), "+v"(q_bim), "+v"(q_cre), "+v"(q_cim), "+v"(q_ldt),
                              "+v"(q_c), "+v"(wmv), "+v"(q_dsk));
            ln1_store();
            SCHED();
            STAMP(1);
            {
                const float cv = bflo(q_c);
                const float s = rbf(cv * sigmoidf_(cv));
                const f32x2 p0 = unbf(wmv.x) * s, p1 = unbf(wmv.y) * s, p2 = unbf(wmv.z) * s, p3 = unbf(wmv.w) * s;
                float a = swap16_add(swap32_add(p0.x, p2.x), swap32_add(p1.x, p3.x));
                float b = swap16_add(swap32_add(p0.y, p2.y), swap32_add(p1.y, p3.y));
                a += dppf<ROR8>(a); b += dppf<ROR8>(b);
                STWS((u32x4{__float_as_uint(a), tag, __float_as_uint(b), tag}), mpw_off);
            }
            u32x4 mp, xt[4];
            gen_tables([&] {
                mp = LDWS(mp_off);
#pragma unroll
                for (int k = 0; k < 4; ++k) xt[k] = LDWS(xt_off + k * (64 * 14336));
                MEMFENCE();
            });
            __syncthreads();
            STAMP(2);
            if (wave < 6) {
                for (u32 spins = 0; !__all(mp.y == tag && mp.w == tag) && spins <= SPIN_CAP; ++spins) {
                    __builtin_amdgcn_s_sleep(1);
                    MEMFENCE();
                    mp = LDWS(mp_off);
                }
                float a = act56 ? __uint_as_float(mp.x) : 0.f, b = act56 ? __uint_as_float(mp.z) : 0.f;
                wave_sum2(a, b);
                if (lane == 0) *(LAS f32x2*)(misc + 16 + 2 * (wave & 1)) = rbf2(f32x2{a, b});
                sub_arrive(cnt + 1, lane);
            }
            sub_wait(cnt + 1, 2);
            STAMP(3);
            const float sa = misc[16 + hch], ca1 = rbf(misc[18 + hch] + 1.f), dsk = bflo(q_dsk);
            const int hexp = max(__builtin_amdgcn_frexp_expf(fmaf(66.f, fabsf(ca1), fabsf(sa) * 1.02f)) - 15, -126);
            const float hsc = pow2c(-hexp);
            if (tid == 256 || tid == 258) ((LAS int*)misc)[24 + hch] = hexp;
            STAMP(4);
            for (u32 spins = 0;; ++spins) {
                bool ok = true;
#pragma unroll
                for (int k = 0; k < 4; ++k) ok = ok && xt[k].y == tag && xt[k].w == tag;
                if (__all(ok) || spins > SPIN_CAP) break;
                MEMFENCE();
#pragma unroll
                for (int k = 0; k < 4; ++k) xt[k] = LDWS(xt_off + k * (64 * 14336));
            }
            STAMP(5);
            {
                const int tb = (t4 >> 2) * 16 + (t4 & 1) * 8;
#pragma unroll
                for (int k = 0; k < 4; ++k) {
                    const f32x2 ha = rbf2(rbf2(unbf(xt[k].x) * ca1) + sa);
                    const f32x2 hb = rbf2(rbf2(unbf(xt[k].z) * ca1) + sa);
                    *(LAS u32x2*)(smem + LDS_HS + hch * HSCH + 512 + tb + k * 1024) = u32x2{pkh(ha * hsc), pkh(hb * hsc)};
                    *(LAS u32x2*)(smem + LDS_HD + hch * 4096 + tb + k * 1024) = u32x2{pkbf(ha * dsk), pkbf(hb * dsk)};
                }
            }
            __syncthreads();
            STAMP(6);
            if (bid == 0 && tid == 256) __hip_atomic_store((GAS u32*)(p.ws + WS_EPOCH), tag, RLX_AGENT);
            STWS(ones, WS_Z + d0 * (2 * L_) + t4 * 16);
            STWS(ones, WS_Z + d0 * (2 * L_) + 4096 + t4 * 16);
            asm volatile("s_waitcnt vmcnt(0)" ::: "memory");
            if (lane == 0) {
                const u32 old = __hip_atomic_fetch_add(cnt + 2, 1u, __ATOMIC_RELAXED, __HIP_MEMORY_SCOPE_WORKGROUP);
                if (old == 3u) __hip_atomic_store((GAS u32*)(p.ws + WS_PD) + tg, tag, RLX_AGENT);
            }
            fetch_modb();
            STAMP(7);
            if (wave == 4) {
                while ((u32)__builtin_amdgcn_readfirstlane(*(volatile LAS u32*)(cnt + 3)) < 4u) __builtin_amdgcn_s_sleep(2);
                check_pd();
            }
            STAMP(19);
        }
        __syncthreads();
        STAMP(8);
    } else {
        u32 q_c = p.c[t448];
        MEMFENCE(); SCHED();
        ln1_compute();
        asm volatile("" : "+v"(q_c));
        ln1_store();
        SCHED();
        STAMP(1);
        STWS(ones, is_mlp ? WS_PART + (u32)(sid * 14336 + tid * 16) : WS_END);
        STWS(ones, is_mlp && tid < 384 ? WS_PART + (u32)(sid * 14336 + 8192 + tid * 16) : WS_END);
        MEMFENCE();
        const int rg = t448 / 7, c7 = t448 - 7 * rg;
        u32x4 wmv[7];
#pragma unroll
        for (int i = 0; i < 7; ++i) wmv[i] = *(const GAS u32x4*)(p.w_mod + (rg + 64 * i) * (6 * D_) + 2 * D_ + 56 * sid + 8 * c7);
        MEMFENCE();
        SCHED();
        {
            LAS float* sv = (LAS float*)(smem + LDS_KD);
            LAS float* part = (LAS float*)(smem + LDS_KD + 2048);
            const float cv = bflo(q_c);
            sv[t448] = rbf(cv * sigmoidf_(cv));
            __syncthreads();
            asm volatile("" : "+v"(wmv[0]));
            f32x2 a[4] = {{0.f, 0.f}, {0.f, 0.f}, {0.f, 0.f}, {0.f, 0.f}};
#pragma unroll
            for (int i = 0; i < 7; ++i) {
                const float s = sv[rg + 64 * i];
#pragma unroll
                for (int e = 0; e < 4; ++e) { const u32 w = wmv[i][e]; a[e] = pfma(unbf(w), (f32x2)s, a[e]); }
            }
            if (tid < D_) {
                *(LAS f32x4*)(part + rg * 56 + 8 * c7) = f32x4{a[0].x, a[0].y, a[1].x, a[1].y};
                *(LAS f32x4*)(part + rg * 56 + 8 * c7 + 4) = f32x4{a[2].x, a[2].y, a[3].x, a[3].y};
            }
            __syncthreads();
            if (tid == 448) __hip_atomic_store((GAS u32*)(p.ws + WS_PD) + tg, tag, RLX_AGENT);
            if (wave < 7) {
                f32x4 lo = *(const LAS f32x4*)(part + lane * 56 + 8 * wave), hi = *(const LAS f32x4*)(part + lane * 56 + 8 * wave + 4);
                float l0 = lo.x, l1 = lo.y, l2 = lo.z, l3 = lo.w, h0 = hi.x, h1 = hi.y, h2 = hi.z, h3 = hi.w;
                wave_sum4(l0, l1, l2, l3); wave_sum4(h0, h1, h2, h3);
                const u32 o0 = pkbf(f32x2{l0, l1}), o1 = pkbf(f32x2{l2, l3}), o2 = pkbf(f32x2{h0, h1}), o3 = pkbf(f32x2{h2, h3});
                if (lane < 2) STWS((lane ? u32x4{o2, tag, o3, tag} : u32x4{o0, tag, o1, tag}), WS_MODB + (28 * sid + 4 * wave + 2 * lane) * 8);
            }
            __syncthreads();
        }
        STAMP(2);
        if (is_mlp) {
#pragma unroll
            for (int i = 0; i < 7; ++i) {
                const int cidx = tid + NT * i, k = cidx >> 3, c8 = cidx & 7;
                __builtin_amdgcn_global_load_lds((const GAS void*)(p.w_gate + k * F_ + 64 * sid + 8 * c8),
                                                 (LAS void*)(smem + (wave + 8 * i) * 1024), 16, 0, 0);
                __builtin_amdgcn_global_load_lds((const GAS void*)(p.w_up + k * F_ + 64 * sid + 8 * c8),
                                                 (LAS void*)(smem + 57344 + (wave + 8 * i) * 1024), 16, 0, 0);
            }
            asm volatile("s_waitcnt vmcnt(0)" ::: "memory");
            __syncthreads();
            {
                const LAS unsigned char* src = smem + ((r16 & 3) >> 1) * 57344 + (8 * g4 + (r16 >> 2)) * 128 + (8 * wave + 4 * (r16 & 1)) * 2;
#pragma unroll
                for (int ks = 0; ks < 14; ++ks) {
                    const u32x2 lo = __builtin_bit_cast(u32x2, __builtin_amdgcn_ds_read_tr16_b64_v4i16((LAS s16x4*)(src + ks * 4096)));
                    const u32x2 hi = __builtin_bit_cast(u32x2, __builtin_amdgcn_ds_read_tr16_b64_v4i16((LAS s16x4*)(src + ks * 4096 + 512)));
                    wgu[ks] = u32x4{lo.x, lo.y, hi.x, hi.y};
                }
            }
            __syncthreads();
#pragma unroll
            for (int i = 0; i < 7; ++i)
                __builtin_amdgcn_global_load_lds((const GAS void*)(p.w_down + 64 * sid * D_ + (tid + NT * i) * 8),
                                                 (LAS void*)(smem + (wave + 8 * i) * 1024), 16, 0, 0);
            asm volatile("s_waitcnt vmcnt(0)" ::: "memory");
            __syncthreads();
            {
                const int w7 = wave < 7 ? wave : 6;
                const LAS unsigned char* src = smem + (8 * g4 + (r16 >> 2)) * 896 + (64 * w7 + 4 * (r16 & 3)) * 2;
#pragma unroll
                for (int i = 0; i < 8; ++i) {
                    const LAS unsigned char* q = src + 32 * (i & 1) * 896 + (i >> 1) * 32;
                    const u32x2 lo = __builtin_bit_cast(u32x2, __builtin_amdgcn_ds_read_tr16_b64_v4i16((LAS s16x4*)(q)));
                    const u32x2 hi = __builtin_bit_cast(u32x2, __builtin_amdgcn_ds_read_tr16_b64_v4i16((LAS s16x4*)(q + 4 * 896)));
                    wdn[i] = u32x4{lo.x, lo.y, hi.x, hi.y};
                }
            }
        }
        STAMP(3);
        if (wave >= 4) fetch_modb();
        if (wave == 0) check_pd();
        __syncthreads();
        STAMP(8);
    }

    const u32x4 wrv = *(const GAS u32x4*)(p.w_r + 8 * l56);
    const u32 zr_off = WS_Z + (u32)(t448 * (2 * L_) + tg * 16);
    STAMP(9);
    f32x2 xv[4], gav[4], sbv[4], cb1[4], gbv[4], cw[4];
    float scw, ssw;
    {
        const u32x4 ga4 = *(const LAS u32x4*)(smem + LDS_MB + l56 * 16);
        const u32x4 sb4 = *(const LAS u32x4*)(smem + LDS_MB + 896 + l56 * 16);
        const u32x4 cb4 = *(const LAS u32x4*)(smem + LDS_MB + 2 * 896 + l56 * 16);
        const u32x4 gb4 = *(const LAS u32x4*)(smem + LDS_MB + 3 * 896 + l56 * 16);
        f32x2 a = {0.f, 0.f}, b = {0.f, 0.f};
#pragma unroll
        for (int i = 0; i < 4; ++i) {
            const u32 x = xrow[i], g = ga4[i], s = sb4[i], c = cb4[i], q = gb4[i], w = wrv[i];
            xv[i] = unbf(x); gav[i] = unbf(g); sbv[i] = unbf(s); cb1[i] = rbf2(unbf(c) + 1.f); gbv[i] = unbf(q);
            cw[i] = act56 ? cb1[i] * unbf(w) : f32x2{0.f, 0.f};
            a += cw[i]; b = pfma(sbv[i], unbf(w), b);
        }
        scw = a.x + a.y; ssw = act56 ? b.x + b.y : 0.f;
        wave_sum2(scw, ssw);
    }
    SCHED();
    asm volatile("s_waitcnt vmcnt(0)" ::: "memory");
    STAMP(22);
    u32x4 z;
    for (u32 spins = 0;; ++spins) {
        MEMFENCE();
        z = LDWS(zr_off);
        if (__all(umax4(z) != ~0u) || spins > SPIN_CAP) break;
        __builtin_amdgcn_s_sleep(1);
    }
    {
        LAS unsigned char* tr = smem + LDS_TR + t448 * 2;
#pragma unroll
        for (int i = 0; i < 4; ++i) {
            const u32 w = z[i];
            *(LAS u16*)(tr + (2 * i) * TRROW) = (u16)w;
            *(LAS u16*)(tr + (2 * i + 1) * TRROW) = (u16)(w >> 16);
        }
    }
    __syncthreads();
    STAMP(10);
    {
        const u32x4 yr = *(const LAS u32x4*)(smem + LDS_TR + wave * TRROW + l56 * 16);
        f32x2 v[4];
#pragma unroll
        for (int i = 0; i < 4; ++i) { const u32 b = yr[i]; v[i] = rbf2(xv[i] + rbf2(unbf(b) * gav[i])); }
        const float pv = __uint_as_float(__builtin_amdgcn_readfirstlane(__float_as_uint(v[0].x)));
        f32x2 s1 = {0.f, 0.f}, s2 = {0.f, 0.f}, s3 = {0.f, 0.f};
#pragma unroll
        for (int i = 0; i < 4; ++i) { const f32x2 dlt = v[i] - pv; s1 += dlt; s2 = pfma(dlt, dlt, s2); s3 = pfma(dlt, cw[i], s3); }
        float a = act56 ? s1.x + s1.y : 0.f, b = act56 ? s2.x + s2.y : 0.f, c = s3.x + s3.y, d = 0.f;
        wave_sum4(a, b, c, d);
        float m, rd;
        ln_finish(pv, a, b, m, rd);
        const float r = fmaf(fmaf(pv - m, scw, c), rd, ssw);
        if (lane == 0) {
            u32 rb = pkbf(f32x2{r, 0.f}) & 0xffffu;
            rb = rb == 0x8000u ? 0u : rb;
            *(LAS u16*)(smem + LDS_MISC + 512 + wave * 2) = (u16)((rb & 0x8000u) ? ~rb : (rb | 0x8000u));
        }
        __syncthreads();
        if (tid < 2) {
            const u32x2 rr = *(const LAS u32x2*)(smem + LDS_MISC + 512 + tid * 8);
            STWS((u32x4{rr.x, tag, rr.y, tag}), WS_RBUF + tg * 32 + tid * 16);
        }
        STAMP(18);
        u32x4 hp, o;
#pragma unroll
        for (int i = 0; i < 4; ++i) {
            const u32 h = pkbf(rbf2(rbf2(rbf2(v[i] - m) * rd) * cb1[i]) + sbv[i]);
            hp[i] = h;
            o[i] = pkbf(v[i] + rbf2(unbf(h) * gbv[i]));
        }
        const __amdgpu_buffer_rsrc_t outr = __builtin_amdgcn_make_buffer_rsrc(p.out, 0, L_ * D_ * 2, 0x00020000);
        if (act56) {
            STWS(hp, WS_H2 + (town * D_ + lane * 8) * 2);
            __builtin_amdgcn_raw_buffer_store_b128(o, outr, (u32)((town * D_ + lane * 8) * 2), 0, 16);
        }
        asm volatile("s_waitcnt vmcnt(0)" ::: "memory");
    }
    __syncthreads();
    if (tid == 0) __hip_atomic_store((GAS u32*)(p.ws + WS_ODONE) + tg, tag, RLX_AGENT);
    STAMP(11);
    if (!is_mlp) return;

    LAS u32* const top = (LAS u32*)(smem + MLP_TOP);
    if (tid < 64) top[128 + tid] = 0u;
    if (tid == 64) top[64] = 0u;
    u32x4 rb;
    for (u32 spins = 0;; ++spins) {
        MEMFENCE();
        rb = LDWS(WS_RBUF + tid * 16);
        if (__all(rb.y == tag && rb.w == tag) || spins > SPIN_CAP) break;
        __builtin_amdgcn_s_sleep(1);
    }
    MEMFENCE();
    const u32x4 odn = LDWS(WS_ODONE + lane * 16);
    MEMFENCE();
    STAMP(12);
    u32 key[4];
    {
        const u32 ib = (u32)(2048 - 4 * tid);
        key[0] = (rb.x << 16) | ib; key[1] = (rb.x & 0xffff0000u) | (ib - 1);
        key[2] = (rb.z << 16) | (ib - 2); key[3] = (rb.z & 0xffff0000u) | (ib - 3);
        const u32 cm = cls_umax(umax(umax(key[0], key[1]), umax(key[2], key[3])));
        if (lane < 8) top[wave * 8 + lane] = cm;
    }
    __syncthreads();
    u32 mykey;
    {
        u32 thr = cls_umax(top[lane]);
        thr = umin(thr, dppu<XOR1>(thr)); thr = umin(thr, dppu<XOR2>(thr)); thr = umin(thr, dppu<HMIR>(thr));
        const u64 m0 = __ballot(key[0] >= thr), m1 = __ballot(key[1] >= thr), m2 = __ballot(key[2] >= thr), m3 = __ballot(key[3] >= thr);
        const u32 n0 = __builtin_popcountll(m0), n1 = __builtin_popcountll(m1), n2 = __builtin_popcountll(m2), n3 = __builtin_popcountll(m3);
        if (n0 + n1 + n2 + n3) {
            u32 base = 0;
            if (lane == 0) base = __hip_atomic_fetch_add(top + 64, n0 + n1 + n2 + n3, __ATOMIC_RELAXED, __HIP_MEMORY_SCOPE_WORKGROUP);
            const u32 p0 = mbcnt64(m0), p1 = mbcnt64(m1) + n0, p2 = mbcnt64(m2) + n0 + n1, p3 = mbcnt64(m3) + n0 + n1 + n2;
            base = __builtin_amdgcn_readfirstlane(base);
            top[128 + (key[0] >= thr ? umin(base + p0, 64u) : 64u)] = key[0];
            top[128 + (key[1] >= thr ? umin(base + p1, 64u) : 64u)] = key[1];
            top[128 + (key[2] >= thr ? umin(base + p2, 64u) : 64u)] = key[2];
            top[128 + (key[3] >= thr ? umin(base + p3, 64u) : 64u)] = key[3];
        }
        __syncthreads();
        const u32 cntv = top[64];
        u32 c = top[128 + (lane & 31)];
        u32x4 q[4];
#pragma unroll
        for (int e = 0; e < 4; ++e) q[e] = *(const LAS u32x4*)(top + 128 + 16 * (lane >> 5) + 4 * e);
        const u32 cnt = __builtin_amdgcn_readfirstlane(cntv);
        if (cnt <= 64u) {
            mykey = 0;
            for (u32 cb = 0;;) {
                u32 rk = 0;
#pragma unroll
                for (int e = 0; e < 4; ++e) rk += (q[e].x > c) + (q[e].y > c) + (q[e].z > c) + (q[e].w > c);
                if (cnt > 32u) {
#pragma unroll
                    for (int e = 0; e < 4; ++e) {
                        const u32x4 q2 = *(const LAS u32x4*)(top + 160 + 16 * (lane >> 5) + 4 * e);
                        rk += (q2.x > c) + (q2.y > c) + (q2.z > c) + (q2.w > c);
                    }
                }
                const auto s = __builtin_amdgcn_permlane32_swap(rk, rk, false, false);
                rk = s[0] + s[1];
                if (wave == 0 && lane < 32 && c && rk < 8u) top[72 + rk] = c;
                const u64 hit = __ballot(c && rk == (u32)wave);
                if (hit) mykey = __builtin_amdgcn_readlane(c, __builtin_ctzll(hit));
                cb += 32;
                if (cb >= cnt) break;
                c = top[160 + (lane & 31)];
            }
        } else {
            mykey = 0;
#pragma unroll 1
            for (int it = 0; it < 8; ++it) {
                const u32 wm = wave_umax(umax(umax(key[0], key[1]), umax(key[2], key[3])));
                __syncthreads();
                if (lane == 0) top[wave] = wm;
                __syncthreads();
                const u32 w = umax(umax4(*(const LAS u32x4*)(top)), umax4(*(const LAS u32x4*)(top + 4)));
#pragma unroll
                for (int i = 0; i < 4; ++i) key[i] = key[i] == w ? 0u : key[i];
                if (tid == 0) top[72 + it] = w;
                mykey = it == wave ? w : mykey;
            }
        }
    }
    STAMP(13);
    {
        const int t = __builtin_amdgcn_readfirstlane(2048 - (int)(mykey & 0xffffu)) & 2047;
        const int e = (t >> 3) & 3;
        u32 f = __builtin_amdgcn_readlane(e == 0 ? odn.x : e == 1 ? odn.y : e == 2 ? odn.z : odn.w, t >> 5);
        for (u32 spins = 0; f != tag && spins <= SPIN_CAP; ++spins) {
            __builtin_amdgcn_s_sleep(1);
            MEMFENCE();
            f = __builtin_amdgcn_readfirstlane(__hip_atomic_load((GAS u32*)(p.ws + WS_ODONE) + (t >> 3), RLX_AGENT));
        }
        MEMFENCE();
        const u32x4 hp = LDWS(WS_H2 + (t * D_ + l56 * 8) * 2);
        *(LAS u32x4*)(smem + MLP_H + wave * TRROW + l56 * 16) = hp;
    }
    __syncthreads();
    STAMP(14);
    const int fj = (tid >> 4) & 7, fc = 16 * sid + (tid & 15);
    const u32 fkey = top[72 + fj];
    const int ft = (2048 - (int)(fkey & 0xffffu)) & 2047;
    const u32 f_y = __hip_atomic_load((GAS u16*)(p.ws + WS_Z) + fc * L_ + ft, RLX_AGENT);
    const u32 f_x = p.x[ft * D_ + fc];
    MEMFENCE();
    {
        f32x4 acc = {0.f, 0.f, 0.f, 0.f};
        const LAS unsigned char* hb = smem + MLP_H + (r16 & 7) * TRROW + g4 * 16;
        bf16x8 hf[14];
#pragma unroll
        for (int ks = 0; ks < 14; ++ks) hf[ks] = *(const LAS bf16x8*)(hb + 64 * ks);
        SCHED();
#pragma unroll
        for (int ks = 0; ks < 14; ++ks)
            acc = __builtin_amdgcn_mfma_f32_16x16x32_bf16(__builtin_bit_cast(bf16x8, wgu[ks]), hf[ks], acc, 0, 0, 0);
        u32x2 o;
#pragma unroll
        for (int k = 0; k < 2; ++k) {
            const u32 mine = pkbf(f32x2{acc[2 * k], acc[2 * k + 1]});
            const auto s = __builtin_amdgcn_permlane32_swap(mine, mine, false, false);
            const f32x2 gv = unbf(s[0]), uv = unbf(s[1]);
            const f32x2 sg = rbf2(f32x2{gv.x * sigmoidf_(gv.x), gv.y * sigmoidf_(gv.y)});
            o[k] = pkbf(sg * uv);
        }
        if (lane < 32 && r16 < 8) *(LAS u32x2*)(smem + MLP_ACT + r16 * 144 + (8 * wave + 4 * g4) * 2) = o;
    }
    __syncthreads();
    STAMP(15);
    if (wave < 7) {
        const LAS unsigned char* ab = smem + MLP_ACT + (r16 & 7) * 144 + g4 * 16;
        const bf16x8 a0 = *(const LAS bf16x8*)(ab), a1 = *(const LAS bf16x8*)(ab + 64);
#pragma unroll
        for (int i = 0; i < 4; ++i) {
            f32x4 acc = {0.f, 0.f, 0.f, 0.f};
            acc = __builtin_amdgcn_mfma_f32_16x16x32_bf16(__builtin_bit_cast(bf16x8, wdn[2 * i]), a0, acc, 0, 0, 0);
            acc = __builtin_amdgcn_mfma_f32_16x16x32_bf16(__builtin_bit_cast(bf16x8, wdn[2 * i + 1]), a1, acc, 0, 0, 0);
            if (r16 < 8) STWS(__builtin_bit_cast(u32x4, acc), WS_PART + ((sid * 28 + 4 * wave + i) * 8 + r16) * 64 + g4 * 16);
        }
        asm volatile("s_waitcnt vmcnt(0)" ::: "memory");
        STAMP(23);
        const int pp = tid >> 5, ck = tid & 31;
        u32x4 a, b;
        for (u32 spins = 0;; ++spins) {
            MEMFENCE();
            a = LDWS(WS_PART + ((2 * pp) * 28 + sid) * 512 + ck * 16);
            b = LDWS(WS_PART + ((2 * pp + 1) * 28 + sid) * 512 + ck * 16);
            if (__all(umax(umax4(a), umax4(b)) != ~0u) || spins > SPIN_CAP) break;
            __builtin_amdgcn_s_sleep(1);
        }
        *(LAS f32x4*)(smem + MLP_RED + pp * 512 + ck * 16) = __builtin_bit_cast(f32x4, a) + __builtin_bit_cast(f32x4, b);
    }
    STAMP(16);
    __syncthreads();
    if (tid < 128) {
        float s = 0.f;
#pragma unroll
        for (int w = 0; w < 14; ++w) s += *(const LAS float*)(smem + MLP_RED + w * 512 + tid * 4);
        const u32 ob = fkey >> 16;
        const float rv = bflo((ob & 0x8000u) ? (ob & 0x7fffu) : (~ob & 0xffffu));
        const float y = rbf(rbf(s) * rbf(sigmoidf_(rv)));
        const float h2 = bflo(*(const LAS u16*)(smem + MLP_H + fj * TRROW + fc * 2));
        const float ga = bflo(*(const LAS u16*)(smem + LDS_MB + fc * 2)), gb = bflo(*(const LAS u16*)(smem + LDS_MB + 3 * 896 + fc * 2));
        const float x1 = rbf(bflo(f_x) + rbf(bflo(f_y) * ga));
        const float o = x1 + rbf(rbf(h2 + y) * gb);
        __hip_atomic_store((GAS u16*)p.out + ft * D_ + fc, (u16)(pkbf(f32x2{o, 0.f}) & 0xffffu), RLX_AGENT);
    }
    STAMP(17);
}

extern "C" void kernel_launch(void* const* d_in, const int* in_sizes, int n_in, void* d_out, int out_size, void* d_ws,
                              size_t ws_size, hipStream_t stream) {
    Params p;
    p.x = (const u16*)d_in[0];
    p.a_re = (const u16*)d_in[1]; p.a_im = (const u16*)d_in[2];
    p.b_re = (const u16*)d_in[3]; p.b_im = (const u16*)d_in[4];
    p.c_re = (const u16*)d_in[5]; p.c_im = (const u16*)d_in[6];
    p.log_dt = (const u16*)d_in[7]; p.dskip = (const u16*)d_in[8];
    p.w_r = (const u16*)d_in[10]; p.w_gate = (const u16*)d_in[11]; p.w_up = (const u16*)d_in[12]; p.w_down = (const u16*)d_in[13];
    p.c = (const u16*)d_in[14]; p.w_mod = (const u16*)d_in[15];
    p.out = (u16*)d_out;
    p.ws = (unsigned char*)d_ws;
    fused_block<<<NWG, NT, 0, stream>>>(p);
}
```

```cpp
#include <hip/hip_runtime.h>
#include <stdint.h>

typedef unsigned int u32;
typedef unsigned long long u64;
typedef unsigned short u16;
typedef float f32x2 __attribute__((ext_vector_type(2)));
typedef float f32x4 __attribute__((ext_vector_type(4)));
typedef _Float16 f16x2 __attribute__((ext_vector_type(2)));
typedef _Float16 f16x8 __attribute__((ext_vector_type(8)));
typedef __bf16 bf16x2 __attribute__((ext_vector_type(2)));
typedef __bf16 bf16x8 __attribute__((ext_vector_type(8)));
typedef u32 u32x2 __attribute__((ext_vector_type(2)));
typedef short s16x4 __attribute__((ext_vector_type(4)));
typedef u32 u32x4 __attribute__((ext_vector_type(4)));

#define GAS __attribute__((address_space(1)))
#define LAS __attribute__((address_space(3)))
#define RLX_AGENT __ATOMIC_RELAXED, __HIP_MEMORY_SCOPE_AGENT
#define MEMFENCE() asm volatile("" ::: "memory")
#define SCHED() __builtin_amdgcn_sched_barrier(0)
#define LDWS(off) __builtin_amdgcn_raw_buffer_load_b128(wsr, (u32)(off), 0, 16)
#define STWS(v, off) __builtin_amdgcn_raw_buffer_store_b128(v, wsr, (u32)(off), 0, 16)

#define MPSLEEP 12
#define PFJ 1
#define GSLEEP 4
#define WSLEEP 16
constexpr int L_ = 2048, D_ = 448, N_ = 256, F_ = 1792;
constexpr int NWG = 256, NT = 512;

constexpr u32 WS_EPOCH = 0;
constexpr u32 WS_MODB = 1024;
constexpr u32 WS_RBUF = WS_MODB + 8192;
constexpr u32 WS_ODONE = WS_RBUF + 8192;
constexpr u32 WS_PD = WS_ODONE + 4096;
constexpr u32 WS_MP = WS_PD + 4096;
constexpr u32 WS_PART = WS_MP + 784 * 512;
constexpr u32 WS_Z = WS_PART + 28 * 14336;
constexpr u32 WS_H2 = WS_Z + D_ * L_ * 2;
constexpr u32 WS_XT = WS_H2 + D_ * L_ * 2;
constexpr u32 WS_END = WS_XT + 256 * 14336;
constexpr u32 WS_DBG = 12u << 20;

constexpr int FROW = 1056;
constexpr int FCH = 53 * FROW;
constexpr int LDS_F = 0;
constexpr int KCOPY = 4160;
constexpr int LDS_KC4 = 0;
constexpr int LDS_XCH = 2 * 4 * KCOPY;
constexpr int LDS_KD = 116 * 1024;
constexpr int KDCH = 1312;
constexpr int LDS_HS = LDS_KD + 2 * KDCH;
constexpr int HSCH = 4608;
constexpr int TRROW = 912;
constexpr int LDS_TR = LDS_HS + 2 * HSCH;
constexpr int LDS_MISC = LDS_TR + 8 * TRROW;
constexpr int LDS_HD = LDS_MISC + 1024;
constexpr int LDS_VEC = LDS_HD + 8192;
constexpr int LDS_TOTAL = LDS_VEC + 5 * 1792;
constexpr int MLP_H = LDS_KD;
constexpr int MLP_ACT = MLP_H + 8 * TRROW;
constexpr int MLP_TOP = MLP_ACT + 8 * 144;
constexpr int MLP_RED = 0;
constexpr int MLP_CAND = 8192;
constexpr int MLP_DN = 32 * 1024;
static_assert(MLP_TOP + 1024 <= LDS_TR, "lds");
static_assert(2 * FCH <= LDS_KD && 2 * 57344 <= LDS_KD && LDS_XCH + 16384 <= LDS_KD && LDS_HD + 14400 <= LDS_TOTAL, "lds");
static_assert(LDS_TOTAL <= 160 * 1024, "lds");
static_assert(LDS_TOTAL > 80 * 1024, "one workgroup per CU");

constexpr u32 SPIN_CAP = 1u << 14;

struct KTab { u16 v[2064]; };
constexpr KTab make_ktab() {
    KTab t{};
    for (int x = 0; x < 2064; ++x) {
        int l = 2047 - x, idx = 641;
        if (l >= 0) {
            int k = l < 256 ? 0 : l < 512 ? 1 : l < 1024 ? 2 : 3;
            int q = l >> k, rem = l & ((1 << k) - 1), half = (1 << k) >> 1;
            if (k > 0 && (rem > half || (rem == half && (q & 1)))) q += 1;
            idx = 128 * k + q;
        }
        t.v[x] = (u16)(2 * idx);
    }
    return t;
}
__device__ const KTab g_ktab __attribute__((aligned(16))) = make_ktab();

struct Params {
    const u16 *x, *a_re, *a_im, *b_re, *b_im, *c_re, *c_im, *log_dt, *dskip, *w_r, *w_gate, *w_up, *w_down, *c, *w_mod;
    u16* out;
    unsigned char* ws;
};

__device__ __forceinline__ float bflo(u32 u) { return __uint_as_float(u << 16); }
__device__ __forceinline__ float bfhi(u32 u) { return __uint_as_float(u & 0xffff0000u); }
__device__ __forceinline__ u32 pkbf(f32x2 a) { return __builtin_bit_cast(u32, __builtin_convertvector(a, bf16x2)); }
__device__ __forceinline__ u32 pkh(f32x2 a) { return __builtin_bit_cast(u32, __builtin_convertvector(a, f16x2)); }
__device__ __forceinline__ f32x2 unbf(u32 u) { return f32x2{bflo(u), bfhi(u)}; }
__device__ __forceinline__ f32x2 rbf2(f32x2 a) { return unbf(pkbf(a)); }
__device__ __forceinline__ float rbf(float a) { return bflo(pkbf(f32x2{a, 0.f})); }
__device__ __forceinline__ f32x2 pfma(f32x2 a, f32x2 b, f32x2 c) { return __builtin_elementwise_fma(a, b, c); }
__device__ __forceinline__ f32x2 cmul(f32x2 a, f32x2 b, f32x2 brot) {
    return pfma(a.yy, brot, a.xx * b);
}
template <int CTRL> __device__ __forceinline__ float dppf(float x) {
    return __builtin_bit_cast(float, __builtin_amdgcn_mov_dpp(__builtin_bit_cast(int, x), CTRL, 0xf, 0xf, true));
}
template <int CTRL> __device__ __forceinline__ u32 dppu(u32 x) {
    return (u32)__builtin_amdgcn_mov_dpp((int)x, CTRL, 0xf, 0xf, true);
}
constexpr int XOR1 = 0xB1, XOR2 = 0x4E, HMIR = 0x141, MIR = 0x140, ROR8 = 0x128;
__device__ __forceinline__ float swap16_add(float a, float b) {
    auto s = __builtin_amdgcn_permlane16_swap(__float_as_uint(a), __float_as_uint(b), false, false);
    return __uint_as_float(s[0]) + __uint_as_float(s[1]);
}
__device__ __forceinline__ float swap32_add(float a, float b) {
    auto s = __builtin_amdgcn_permlane32_swap(__float_as_uint(a), __float_as_uint(b), false, false);
    return __uint_as_float(s[0]) + __uint_as_float(s[1]);
}
__device__ __forceinline__ float row_sum(float x) {
    x += dppf<XOR1>(x); x += dppf<XOR2>(x); x += dppf<HMIR>(x); x += dppf<MIR>(x);
    return x;
}
__device__ __forceinline__ float wave_sum(float x) {
    x = row_sum(x);
    x = swap16_add(x, x);
    return swap32_add(x, x);
}
__device__ __forceinline__ float rdl(float x, int l) { return __uint_as_float(__builtin_amdgcn_readlane(__float_as_uint(x), l)); }
__device__ __forceinline__ void wave_sum4(float& a, float& b, float& c, float& d) {
    const float x = row_sum(swap16_add(swap32_add(a, c), swap32_add(b, d)));
    a = rdl(x, 0); b = rdl(x, 16); c = rdl(x, 32); d = rdl(x, 48);
}
__device__ __forceinline__ void wave_sum2(float& a, float& b) {
    const float y = swap32_add(a, b);
    const float x = row_sum(swap16_add(y, y));
    a = rdl(x, 0); b = rdl(x, 32);
}
__device__ __forceinline__ u32 umax(u32 a, u32 b) { return a > b ? a : b; }
__device__ __forceinline__ u32 umin(u32 a, u32 b) { return a < b ? a : b; }
__device__ __forceinline__ u32 cls_umax(u32 x) {
    x = umax(x, dppu<ROR8>(x));
    auto s = __builtin_amdgcn_permlane16_swap(x, x, false, false);
    x = umax(s[0], s[1]);
    auto t = __builtin_amdgcn_permlane32_swap(x, x, false, false);
    return umax(t[0], t[1]);
}
__device__ __forceinline__ u32 wave_umax(u32 x) {
    x = umax(x, dppu<XOR1>(x)); x = umax(x, dppu<XOR2>(x)); x = umax(x, dppu<HMIR>(x));
    return cls_umax(x);
}
__device__ __forceinline__ u32 mbcnt64(u64 m) {
    return __builtin_amdgcn_mbcnt_hi((u32)(m >> 32), __builtin_amdgcn_mbcnt_lo((u32)m, 0u));
}
__device__ __forceinline__ u32 umax4(u32x4 v) { return umax(umax(v.x, v.y), umax(v.z, v.w)); }
__device__ __forceinline__ float pow2i(int e) {
    e = e > 127 ? 127 : e;
    return e < -126 ? 0.f : __uint_as_float((u32)(e + 127) << 23);
}
__device__ __forceinline__ float pow2c(int e) {
    e = e > 127 ? 127 : e < -126 ? -126 : e;
    return __uint_as_float((u32)(e + 127) << 23);
}
__device__ __forceinline__ float sigmoidf_(float x) { return __builtin_amdgcn_rcpf(1.f + __expf(-x)); }
__device__ __forceinline__ void ln_finish(float pv, float s1, float s2, float& m, float& rd) {
    const float mu = s1 * (1.f / D_);
    const float var = fmaxf(fmaf(-mu, mu, s2 * (1.f / D_)), 0.f);
    m = rbf(pv + mu);
    rd = __builtin_amdgcn_rcpf(rbf(__builtin_amdgcn_sqrtf(rbf(rbf(var) + 1e-5f))));
}

__device__ __forceinline__ void sub_arrive(LAS u32* cnt, int lane) {
    MEMFENCE();
    if (lane == 0) __hip_atomic_fetch_add(cnt, 1u, __ATOMIC_RELAXED, __HIP_MEMORY_SCOPE_WORKGROUP);
}
__device__ __forceinline__ void sub_wait(LAS u32* cnt, u32 n) {
    while ((u32)__builtin_amdgcn_readfirstlane(*(volatile LAS u32*)cnt) < n) { }
    MEMFENCE();
}
__device__ __forceinline__ float exp_acc(float x) {
    const float t = x * 1.44269504088896341f;
    const float e = fmaf(x, 1.9259630e-08f, fmaf(x, 1.44269504088896341f, -t));
    const float r = __builtin_amdgcn_exp2f(t);
    return fmaf(r, e * 0.69314718056f, r);
}

#define STAMP(i) do { } while (0)
#define STAMP_INIT() do { } while (0)
#define STAMP_DUMP() do { } while (0)
#define LDS_EXTRA 0
#define CODE_TAIL 0

template <int VH, typename Pre, typename Send>
__device__ __forceinline__ void conv_all(const LAS unsigned char* pa, const LAS unsigned char* pa2, const LAS unsigned char* pb,
                                         f32x4 (&acc)[8], Pre pre, Send send) {
    u32x2 Alo[2][8], Ahi[2][8];
    f16x8 B[2][8];
#pragma unroll
    for (int i = 0; i < 8; ++i) {
        Alo[0][i] = *(const LAS u32x2*)(pa + 192 + 512 * (7 - i));
        Ahi[0][i] = *(const LAS u32x2*)(pa2 + 192 + 512 * (7 - i));
        B[0][i] = *(const LAS f16x8*)(pb + 192 + 512 * i);
        SCHED();
    }
#pragma unroll
    for (int vi = 0; vi < 4; ++vi) {
        const int cur = vi & 1, nxt = cur ^ 1;
        int n = 0;
#pragma unroll
        for (int part = 0; part < (vi == 3 ? 2 : 1); ++part) {
#pragma unroll
            for (int s = 0; s < 8; ++s) {
#pragma unroll
                for (int a = 0; a < 8; ++a) {
#pragma unroll
                    for (int b = 0; b < 8; ++b) {
                        const int k = (a + b) ^ (4 * VH);
                        if (a + b < 8 && (a > b ? a : b) == s && (vi < 3 || (k >= 4) == (part == 0))) {
                            const f16x8 Af = __builtin_bit_cast(f16x8, u32x4{Alo[cur][b].x, Alo[cur][b].y, Ahi[cur][b].x, Ahi[cur][b].y});
                            acc[k] = __builtin_amdgcn_mfma_f32_16x16x32_f16(Af, B[cur][a], acc[k], 0, 0, 0);
                            if (vi < 3 && n >= 4 && n % 3 == 1 && (n - 4) / 3 < 8) {
                                const int i = (n - 4) / 3, off = 192 - 64 * (vi + 1);
                                Alo[nxt][i] = *(const LAS u32x2*)(pa + off + 512 * (7 - i));
                                Ahi[nxt][i] = *(const LAS u32x2*)(pa2 + off + 512 * (7 - i));
                                B[nxt][i] = *(const LAS f16x8*)(pb + off + 512 * i);
                            }
                            if (vi == 2 && n == 28) pre();
                            if (vi == 3 && part == 1 && n == (VH ? 12 : 28)) send();
                            SCHED();
                            ++n;
                        }
                    }
                }
            }
        }
    }
}

__global__ void __launch_bounds__(NT, 2) fused_block(Params p) {
    __shared__ __attribute__((aligned(16))) unsigned char smem_[LDS_TOTAL + LDS_EXTRA];
    LAS unsigned char* const smem = (LAS unsigned char*)smem_;
    const int tid = threadIdx.x, lane = tid & 63;
    const int wave = __builtin_amdgcn_readfirstlane(tid >> 6);
    const int bid = blockIdx.x;
    const int xg = bid & 7, xi = bid >> 3;
    const bool is_conv = xi < 28;
    const int pair = xg * 28 + xi, d0 = 2 * pair;
    const int sid = xg * 4 + (xi - 28);
    const bool is_mlp = !is_conv && sid < 28;
    const int tg = xg * 32 + xi;
    const int town = 8 * tg + wave;
    const __amdgpu_buffer_rsrc_t wsr = __builtin_amdgcn_make_buffer_rsrc(p.ws, 0, WS_END, 0x00020000);
    const int r16 = lane & 15, g4 = lane >> 4;
    const bool act56 = lane < 56;
    const int l56 = act56 ? lane : 55;
    const int t448 = tid < D_ ? tid : D_ - 1;
    const u32x4 ones = {~0u, ~0u, ~0u, ~0u};
    LAS float* const misc = (LAS float*)(smem + LDS_MISC);
    STAMP_INIT();
    STAMP(0);

    u32x4 xrow = *(const GAS u32x4*)(p.x + town * D_ + l56 * 8);
    u32 tag = __hip_atomic_load((GAS u32*)(p.ws + WS_EPOCH), RLX_AGENT);
    u64 code_pc; int code_len;
    asm volatile(".Lfb_code_begin:\n\ts_getpc_b64 %0\n\ts_mov_b32 %1, .Lfb_code_end-.Lfb_code_begin" : "=s"(code_pc), "=s"(code_len));
    const u32 pfc = *(const GAS u32*)((code_pc & ~3ull) + (u64)(min((xi * 8 + wave + (lane >> 5) * 256) * 128 + (lane & 31) * 4, code_len + CODE_TAIL)));
    MEMFENCE();
    u32x4 xtp[2];
    auto ln1_compute = [&](auto arrived, auto pre_barrier) {
        asm volatile("" : "+v"(xrow), "+v"(tag));
        tag = __builtin_amdgcn_readfirstlane(tag) + 1u;
        STAMP(20);
        arrived();
        const f32x2 v[4] = {unbf(xrow.x), unbf(xrow.y), unbf(xrow.z), unbf(xrow.w)};
        const float pv = __uint_as_float(__builtin_amdgcn_readfirstlane(xrow.x << 16));
        f32x2 s1 = {0.f, 0.f}, s2 = {0.f, 0.f};
#pragma unroll
        for (int i = 0; i < 4; ++i) { const f32x2 dlt = v[i] - pv; s1 += dlt; s2 = pfma(dlt, dlt, s2); }
        float a = act56 ? s1.x + s1.y : 0.f, b = act56 ? s2.x + s2.y : 0.f;
        wave_sum2(a, b);
        float m, rd;
        ln_finish(pv, a, b, m, rd);
        u32x4 xn;
#pragma unroll
        for (int i = 0; i < 4; ++i) xn[i] = pkbf(rbf2(v[i] - m) * rd);
        *(LAS u32x4*)(smem + LDS_TR + wave * TRROW + l56 * 16) = xn;
        const LAS unsigned char* src = smem + LDS_TR + (tid & 1) * 4 * TRROW + (tid >> 1) * 2;
        pre_barrier();
        __syncthreads();
        STAMP(21);
#pragma unroll
        for (int k = 0; k < 2; ++k) {
            const u32 t0 = *(const LAS u16*)(src + 512 * k), t1 = *(const LAS u16*)(src + 512 * k + TRROW);
            const u32 t2 = *(const LAS u16*)(src + 512 * k + 2 * TRROW), t3 = *(const LAS u16*)(src + 512 * k + 3 * TRROW);
            xtp[k] = u32x4{t0 | (t1 << 16), tag, t2 | (t3 << 16), tag};
        }
    };
    auto ln1_store = [&]() {
        const u32 off = WS_XT + (u32)(tg * 14336 + tid * 16);
        STWS(xtp[0], off);
        STWS(xtp[1], tid < 384 ? off + 8192u : WS_END);
    };

    auto build_vec = [&]() {
        const int t = min(tid - 256, 223);
        const u32 wr = *(const GAS u32*)(p.w_r + 2 * t);
        u64 g[4];
        for (u32 spins = 0;; ++spins) {
            MEMFENCE();
#pragma unroll
            for (int k = 0; k < 4; ++k) g[k] = __hip_atomic_load((GAS u64*)(p.ws + WS_MODB) + k * 224 + t, RLX_AGENT);
            if (__all((u32)(g[0] >> 32) == tag && (u32)(g[1] >> 32) == tag && (u32)(g[2] >> 32) == tag && (u32)(g[3] >> 32) == tag)
                || spins > SPIN_CAP) break;
            __builtin_amdgcn_s_sleep(16);
        }
        const f32x2 cb1 = rbf2(unbf((u32)g[2]) + 1.f), sb = unbf((u32)g[1]), cw = cb1 * unbf(wr), sw = sb * unbf(wr);
        LAS f32x2* v = (LAS f32x2*)(smem + LDS_VEC) + t;
        v[0] = unbf((u32)g[0]); v[224] = cw; v[448] = cb1; v[672] = sb; v[896] = unbf((u32)g[3]);
        float a = tid < 480 ? cw.x + cw.y : 0.f, b = tid < 480 ? sw.x + sw.y : 0.f;
        wave_sum2(a, b);
        if (lane == 0) *(LAS f32x2*)(misc + 40 + 2 * (wave - 4)) = f32x2{a, b};
    };
    auto check_pd = [&]() {
        for (u32 spins = 0;; ++spins) {
            MEMFENCE();
            const u32x4 pd = LDWS(WS_PD + min(tid - 256, 223) * 16);
            if (__all(pd.x == tag && pd.y == tag && pd.z == tag && pd.w == tag) || spins > SPIN_CAP) break;
            __builtin_amdgcn_s_sleep(8);
        }
    };

    u32x4 wgu[14];
    u32x4 wdn[8];

    if (is_conv) {
        LAS u32* const cnt = (LAS u32*)(misc + 28);
        const int kc = (wave >> 1) & 1;
        const int o = d0 * N_ + (tid & 255) * 2;
        const u32 q_are = *(const GAS u32*)(p.a_re + o), q_aim = *(const GAS u32*)(p.a_im + o);
        const u32 q_ldt = p.log_dt[d0 + kc];
        LAS unsigned char* const fw = smem + LDS_F + kc * FCH + (tid & 127) * 8;
        auto put = [&](int row, f32x2 v0, f32x2 v1) { *(LAS u32x2*)(fw + row * FROW) = u32x2{pkh(v0), pkh(v1)}; };
        float dt, gw;
        f32x2 a2, ph, pe;
        auto rates = [&]() {
            dt = rbf(exp_acc(bflo(q_ldt)));
            f32x2 lr = unbf(q_are) * dt;
            const f32x2 li = unbf(q_aim) * dt;
            gw = 0.f;
            if (__builtin_expect(__any(lr.x > 0.f || lr.y > 0.f), 0)) {
                gw = __uint_as_float(wave_umax(__float_as_uint(fmaxf(fmaxf(lr.x, lr.y), 0.f))));
                lr -= gw;
            }
            a2 = lr * 1.44269504088896341f;
            ph = li * 0.15915494309189535f;
            pe = pfma(li, (f32x2)6.4206383e-09f, pfma(li, (f32x2)0.15915494309189535f, -ph));
        };
        auto anchor = [&](float m, float sgn, f32x2& e0, f32x2& e1) {
            const f32x2 am = a2 * m, pm = ph * m;
            const f32x2 rev = pfma(pe, (f32x2)m, f32x2{__builtin_amdgcn_fractf(pm.x), __builtin_amdgcn_fractf(pm.y)}) * sgn;
            e0 = f32x2{__builtin_amdgcn_cosf(rev.x), __builtin_amdgcn_sinf(rev.x)} * __builtin_amdgcn_exp2f(am.x);
            e1 = f32x2{__builtin_amdgcn_cosf(rev.y), __builtin_amdgcn_sinf(rev.y)} * __builtin_amdgcn_exp2f(am.y);
        };

        if (wave < 4) {
            u32 q_bre = *(const GAS u32*)(p.b_re + o);
            MEMFENCE();
            const u32 q_bim = *(const GAS u32*)(p.b_im + o), q_cre = *(const GAS u32*)(p.c_re + o), q_cim = *(const GAS u32*)(p.c_im + o);
            const u32x2 kt1 = *(const u32x2*)(g_ktab.v + 8 * tid + 8);
            const u32x4 kt0 = *(const u32x4*)(g_ktab.v + 8 * tid);
            MEMFENCE();
            if (tid < 64) *(LAS u32x4*)(smem + LDS_HS + (tid >> 5) * HSCH + (tid & 31) * 16) = u32x4{0u, 0u, 0u, 0u};
            if (tid >= 64 && tid < 66) *(LAS u16*)(smem + LDS_KD + (tid - 64) * KDCH + 2 * 641) = 0;
            if (tid >= 66 && tid < 68) *(LAS u32x4*)(cnt + 4 * (tid - 66)) = u32x4{0u, 0u, 0u, 0u};
            if (tid >= 68 && tid < 70) *(LAS u32x4*)(misc + 48 + 4 * (tid - 68)) = u32x4{0u, 0u, 0u, 0u};
            const int xw = min(xg, 6);
            const u16* pfp = (wave == 0 ? p.w_gate + 16 * xi * F_ + 256 * xw : wave == 1 ? p.w_up + 16 * xi * F_ + 256 * xw
                              : wave == 2 ? p.w_down + 256 * xw * D_ + 4096 * xi : p.w_mod + 16 * xi * (6 * D_) + 2 * D_ + 224 * xg)
                             + (lane >> 2) * (wave < 2 ? F_ : wave == 2 ? 256 : 6 * D_) + (lane & 3) * (wave < 3 ? 64 : 74);
            u32 pf1 = 0;
            if (wave == 3) pf1 = *(const GAS u32*)pfp;
            const int ta = wave & 1;
            int ibase, io1, io2, iob;
            {
                const int cs = g4 < 2 ? 8 : g4 == 2 ? 4 : 2;
                const int ib0 = (g4 < 2 ? 4 * g4 : g4 == 2 ? 256 : 384) + cs * r16;
                const bool v1 = g4 == 0, v20 = lane == 16;
                ibase = ta ? (v1 ? 512 + r16 : v20 ? 640 : 644) : ib0;
                io1 = ta ? (v1 ? 32 : v20 ? 4 : 0) : 1;
                io2 = ta ? (v1 ? 64 : v20 ? 5 : 0) : (g4 == 3 ? 64 : 2);
                iob = ta ? (v1 ? 16 : v20 ? 4 : 0) : 16 * cs;
            }
            LAS unsigned char* kda[8];
#pragma unroll
            for (int i = 0; i < 8; ++i) {
                kda[i] = smem + LDS_KD + kc * KDCH + 2 * (ibase + (i & 1) * io1 + ((i >> 1) & 1) * io2 + (i >> 2) * iob);
                asm volatile("" : "+v"(kda[i]));
            }
            SCHED();
            ln1_compute([&] { if (PFJ == -1) { if (wave < 2) pf1 = *(const GAS u32*)pfp; MEMFENCE(); } },
                        [&] { if (PFJ == -2) { if (wave < 2) pf1 = *(const GAS u32*)pfp; MEMFENCE(); } });
            ln1_store();
            SCHED();
            STAMP(1);
            int kexp;
            {
                rates();
                const f32x2 bre = unbf(q_bre) * dt, bim = unbf(q_bim) * dt, cre = unbf(q_cre), cim = unbf(q_cim);
                const f32x2 cr = pfma(cre, bre, -(cim * bim)), ci = pfma(cre, bim, cim * bre);
                const u32 mw = wave_umax(umax(umax(__float_as_uint(cr.x) & 0x7fffffffu, __float_as_uint(ci.x) & 0x7fffffffu),
                                              umax(__float_as_uint(cr.y) & 0x7fffffffu, __float_as_uint(ci.y) & 0x7fffffffu)));
                if (lane == 0) { ((LAS u32*)misc)[wave] = mw; misc[8 + wave] = gw; }
                sub_arrive(cnt + 6 + kc, lane);
                f32x2 e8[2], e64[2];
                anchor(8.f, -1.f, e8[0], e8[1]);
                anchor(64.f, -1.f, e64[0], e64[1]);
                sub_wait(cnt + 6 + kc, 2);
                const u32x2 mwv = *(const LAS u32x2*)(misc + 2 * kc);
                const int emax = __builtin_amdgcn_frexp_expf(__uint_as_float(umax(mwv.x, mwv.y)));
                kexp = __builtin_amdgcn_readfirstlane(emax) - 6;
                f32x2 b[2][4];
#pragma unroll
                for (int m = 0; m < 2; ++m) {
                    const f32x2 er = {-e64[m].y, e64[m].x};
                    b[m][0] = f32x2{__builtin_amdgcn_ldexpf(cr[m], 14 - emax), -__builtin_amdgcn_ldexpf(ci[m], 14 - emax)};
#pragma unroll
                    for (int q = 1; q < 4; ++q) b[m][q] = cmul(b[m][q - 1], e64[m], er);
                }
                const f32x2 e8r[2] = {{-e8[0].y, e8[0].x}, {-e8[1].y, e8[1].x}};
#pragma unroll
                for (int j = 0; j < 8; ++j) {
#pragma unroll
                    for (int q = 0; q < 4; ++q) {
                        put(21 + 8 * q + j, b[0][q], b[1][q]);
                        if (j < 7) { b[0][q] = cmul(b[0][q], e8[0], e8r[0]); b[1][q] = cmul(b[1][q], e8[1], e8r[1]); }
                    }
                    if (j == PFJ) { if (wave < 2) pf1 = *(const GAS u32*)pfp; MEMFENCE(); }
                }
            }
            sub_arrive(cnt + 2, lane);
            sub_wait(cnt + 2, 8);
            STAMP(2);

            {
                const LAS unsigned char* pa = smem + LDS_F + kc * FCH + (16 * ta + r16) * FROW + g4 * 16;
                const LAS unsigned char* pb = smem + LDS_F + kc * FCH + (21 + r16) * FROW + g4 * 16;
                const f32x2 gwv = *(const LAS f32x2*)(misc + 8 + 2 * kc);
                f16x8 fa[16], fb0[16], fb1[16];
#pragma unroll
                for (int ks = 0; ks < 8; ++ks) {
                    fa[ks] = *(const LAS f16x8*)(pa + 64 * ks); fb0[ks] = *(const LAS f16x8*)(pb + 64 * ks);
                    fb1[ks] = *(const LAS f16x8*)(pb + 16 * FROW + 64 * ks);
                }
                SCHED();
                const u32 gany = __builtin_amdgcn_readfirstlane(__float_as_uint(gwv.x) | __float_as_uint(gwv.y));
                f32x4 val[2];
                {
                    val[0] = f32x4{0.f, 0.f, 0.f, 0.f}; val[1] = val[0];
#pragma unroll
                    for (int ks = 0; ks < 16; ++ks) {
                        val[0] = __builtin_amdgcn_mfma_f32_16x16x32_f16(fa[ks], fb0[ks], val[0], 0, 0, 0);
                        val[1] = __builtin_amdgcn_mfma_f32_16x16x32_f16(fa[ks], fb1[ks], val[1], 0, 0, 0);
                        if (ks < 8) {
                            fa[ks + 8] = *(const LAS f16x8*)(pa + 64 * (ks + 8)); fb0[ks + 8] = *(const LAS f16x8*)(pb + 64 * (ks + 8));
                            fb1[ks + 8] = *(const LAS f16x8*)(pb + 16 * FROW + 64 * (ks + 8));
                        }
                        SCHED();
                    }
                }
                if (__builtin_expect(gany != 0u, 0)) {
                    const float gmax = fmaxf(gwv.x, gwv.y);
                    const float G = fminf(ceilf(gmax * (2048.f * 1.44269504088896341f)), 1000.f);
                    kexp += __builtin_amdgcn_readfirstlane((int)G);
                    val[0] = f32x4{0.f, 0.f, 0.f, 0.f}; val[1] = val[0];
#pragma unroll 1
                    for (int w4 = 0; w4 < 4; ++w4) {
                        f32x4 a0 = {0.f, 0.f, 0.f, 0.f}, a1 = a0;
#pragma unroll
                        for (int k4 = 0; k4 < 4; ++k4) {
                            const f16x8 af = *(const LAS f16x8*)(pa + 256 * w4 + 64 * k4);
                            a0 = __builtin_amdgcn_mfma_f32_16x16x32_f16(af, *(const LAS f16x8*)(pb + 256 * w4 + 64 * k4), a0, 0, 0, 0);
                            a1 = __builtin_amdgcn_mfma_f32_16x16x32_f16(af, *(const LAS f16x8*)(pb + 16 * FROW + 256 * w4 + 64 * k4), a1, 0, 0, 0);
                        }
                        const float g2 = (w4 < 2 ? gwv.x : gwv.y) * 1.44269504088896341f;
#pragma unroll
                        for (int i = 0; i < 8; ++i) {
                            const int ix = min(ibase + (i & 1) * io1 + ((i >> 1) & 1) * io2 + (i >> 2) * iob, 640);
                            const int k = ix < 256 ? 0 : ix < 384 ? 1 : ix < 512 ? 2 : 3;
                            const float f = __builtin_amdgcn_exp2f(fmaf(g2, (float)((ix - 128 * k) << k), -G));
                            val[i >> 2][i & 3] = fmaf((i >> 2) ? a1[i & 3] : a0[i & 3], f, val[i >> 2][i & 3]);
                        }
                    }
                }
#pragma unroll
                for (int tb = 0; tb < 2; ++tb) {
                    const u32 k01 = pkh(val[tb].xy * 0x1p-23f), k23 = pkh(val[tb].zw * 0x1p-23f);
                    *(LAS u16*)kda[4 * tb] = (u16)k01; *(LAS u16*)kda[4 * tb + 1] = (u16)(k01 >> 16);
                    *(LAS u16*)kda[4 * tb + 2] = (u16)k23; *(LAS u16*)kda[4 * tb + 3] = (u16)(k23 >> 16);
                }
            }
            sub_arrive(cnt, lane);
            const u32 tb[6] = {kt0.x, kt0.y, kt0.z, kt0.w, kt1.x, kt1.y};
            u32 tlo[6], thi[6];
#pragma unroll
            for (int i = 0; i < 6; ++i) { tlo[i] = tb[i] & 0xffffu; thi[i] = tb[i] >> 16; }
            sub_wait(cnt, 4);
            STAMP(3);

#pragma unroll
            for (int c2 = 0; c2 < 2; ++c2) {
                const LAS unsigned char* kd = smem + LDS_KD + c2 * KDCH;
                u32 rg[6];
#pragma unroll
                for (int i = 0; i < 6; ++i) {
                    const u32 lo = *(const LAS u16*)(kd + tlo[i]);
                    const u32 hi = *(const LAS u16*)(kd + thi[i]);
                    rg[i] = lo | (hi << 16);
                }
                u32 al[5];
#pragma unroll
                for (int i = 0; i < 5; ++i) al[i] = __builtin_amdgcn_alignbit(rg[i + 1], rg[i], 16);
                LAS unsigned char* kcp = smem + LDS_KC4 + c2 * 4 * KCOPY + tid * 16;
                *(LAS u32x4*)(kcp) = u32x4{rg[0], rg[1], rg[2], rg[3]};
                *(LAS u32x4*)(kcp + KCOPY) = u32x4{al[0], al[1], al[2], al[3]};
                *(LAS u32x4*)(kcp + 2 * KCOPY) = u32x4{rg[1], rg[2], rg[3], rg[4]};
                *(LAS u32x4*)(kcp + 3 * KCOPY) = u32x4{al[1], al[2], al[3], al[4]};
            }
            if (tid < 16) *(LAS u32x4*)(smem + LDS_KC4 + (tid >> 1) * KCOPY + 4096 + (tid & 1) * 16) = u32x4{0u, 0u, 0u, 0u};
            const int vh = ta;
            const LAS unsigned char* pa = smem + LDS_KC4 + (kc * 4 + 3 - (r16 & 3)) * KCOPY
                                          + 2 * (2044 - 256 * 7 - 16 * (8 * vh + 7) - 4 * (r16 >> 2) + 8 * g4);
            const LAS unsigned char* pb = smem + LDS_HS + kc * HSCH + 512 - 32 * (8 * vh + 7) + 32 * r16 + 16 * g4;
            const LAS unsigned char* pa2 = pa + 8;
            asm volatile("" : "+v"(pa2));
            u32 z_off = WS_Z + (u32)((d0 + kc) * (2 * L_) + 2048 * vh + 512 * (g4 & 1) + 32 * r16 + 16 * (g4 >> 1));
            asm volatile("" : "+v"(z_off));
            STAMP(4);
            __syncthreads();
            STAMP(6);
            asm volatile("" :: "v"(pf1));
            if (wave == 2 || (PFJ == 9 && wave < 2)) pf1 = *(const GAS u32*)pfp;

            f32x4 acc[8];
#pragma unroll
            for (int k = 0; k < 8; ++k) acc[k] = f32x4{0.f, 0.f, 0.f, 0.f};
            u32x2 hdv[4];
            int hexp;
            LAS u32* const flg = (LAS u32*)(misc + 48);
            LAS f32x4* const xw4 = (LAS f32x4*)(smem + LDS_XCH + wave * 4096) + lane;
            auto pre = [&] {
#pragma unroll
                for (int k = 0; k < 4; ++k) hdv[k] = *(const LAS u32x2*)(smem + LDS_HD + kc * 4096 + 2048 * vh + 512 * k + 32 * r16 + 8 * g4);
                hexp = ((LAS int*)misc)[24 + kc];
            };
            auto send = [&] {
#pragma unroll
                for (int k = 0; k < 4; ++k) xw4[64 * k] = acc[4 + k];
                MEMFENCE();
                *(volatile LAS u32*)(flg + wave) = 1u;
            };
            if (vh) conv_all<1>(pa, pa2, pb, acc, pre, send); else conv_all<0>(pa, pa2, pb, acc, pre, send);
            STAMP(7);
            const int ye = __builtin_amdgcn_readfirstlane(kexp + hexp), yc = max(min(ye, 127), -126);
            const float ys0 = __uint_as_float((u32)(yc + 127) << 23);
            for (;;) {
                const u32 f0 = *(volatile LAS u32*)(flg + (wave ^ 1));
                const u32x4 f1 = *(volatile LAS u32x4*)(flg + 4);
                if (__builtin_amdgcn_readfirstlane(f0 & f1.x & f1.y & f1.z & f1.w)) break;
            }
            MEMFENCE();
            const LAS f32x4* xr = (const LAS f32x4*)(smem + LDS_XCH + (wave ^ 1) * 4096) + lane;
            f32x4 y[4];
#pragma unroll
            for (int k = 0; k < 4; ++k) y[k] = xr[64 * k];
            SCHED();
#pragma unroll
            for (int k = 0; k < 4; ++k) y[k] = (y[k] + acc[k]) * ys0;
            if (__builtin_expect(ye != yc, 0)) {
                const float ys1 = pow2c(ye - yc);
#pragma unroll
                for (int k = 0; k < 4; ++k) y[k] *= ys1;
            }
#pragma unroll
            for (int k2 = 0; k2 < 2; ++k2) {
                const f32x4 ya = y[2 * k2], yb = y[2 * k2 + 1];
                const u32 a0 = pkbf(rbf2(ya.xy) + unbf(hdv[2 * k2].x)), a1 = pkbf(rbf2(ya.zw) + unbf(hdv[2 * k2].y));
                const u32 b0 = pkbf(rbf2(yb.xy) + unbf(hdv[2 * k2 + 1].x)), b1 = pkbf(rbf2(yb.zw) + unbf(hdv[2 * k2 + 1].y));
                const auto s0 = __builtin_amdgcn_permlane16_swap(a0, b0, false, false);
                const auto s1 = __builtin_amdgcn_permlane16_swap(a1, b1, false, false);
                STWS((u32x4{s0[0], s1[0], s0[1], s1[1]}), z_off + 1024u * k2);
            }
            STAMP(19);
            asm volatile("" :: "v"(pf1));
            sub_wait(cnt + 3, 4);
        } else {
            const int t4 = tid - 256;
            __builtin_amdgcn_s_setprio(1);
            const int unit = min(pair * 4 + (wave & 3), 783), ucb = unit / 56, urow = (unit - ucb * 56) * 8 + (lane >> 3);
            const int hch = (lane >> 1) & 1;
            u32 q_dsk = p.dskip[d0 + hch];
            MEMFENCE();
            u32 q_c = p.c[urow];
            u32x4 wmv = *(const GAS u32x4*)(p.w_mod + urow * (6 * D_) + ucb * 64 + (lane & 7) * 8);
            MEMFENCE();
            const u32 xt_off = WS_XT + (u32)((t4 >> 2) * 14336 + d0 * 32 + (t4 & 3) * 16);
            const int mcol = (wave & 1) * D_ + d0;
            const u32 mp_off = wave < 6 ? WS_MP + (u32)(((mcol >> 6) * 56 + l56) * 512 + ((mcol & 63) >> 1) * 16) : WS_END;
            const u32 mpw_off = ((lane & 8) || pair * 4 + (wave & 3) >= 784) ? WS_END : WS_MP + (u32)(unit * 512 + ((lane & 7) * 4 + g4) * 16);
            SCHED();
            ln1_compute([] {}, [] {});
            ln1_store();
            SCHED();
            STAMP(1);
            {
                rates();
                f32x2 e1[2], e256[2], s[2][8], u[2], v[2];
                anchor(1.f, 1.f, e1[0], e1[1]);
                anchor(256.f, 1.f, e256[0], e256[1]);
                const f32x2 e1r[2] = {{-e1[0].y, e1[0].x}, {-e1[1].y, e1[1].x}};
                const f32x2 e2r[2] = {{-e256[0].y, e256[0].x}, {-e256[1].y, e256[1].x}};
                *(LAS u32x2*)fw = u32x2{0x00007800u, 0x00007800u};
#pragma unroll
                for (int m = 0; m < 2; ++m) { s[m][1] = e1[m] * 32768.f; u[m] = e256[m] * 32768.f; }
                put(1, s[0][1], s[1][1]); put(8, u[0], u[1]);
                constexpr int UROW[7] = {12, 14, 16, 17, 18, 19, 20};
#pragma unroll
                for (int e = 2; e < 9; ++e) {
#pragma unroll
                    for (int m = 0; m < 2; ++m) {
                        if (e < 8) s[m][e] = cmul(s[m][e - 1], e1[m], e1r[m]);
                        u[m] = cmul(u[m], e256[m], e2r[m]);
                    }
                    if (e < 8) put(e, s[0][e], s[1][e]);
                    put(UROW[e - 2], u[0], u[1]);
                    if (e == 2 || e == 6) put(8 + e / 2, cmul(s[0][e], e256[0], e2r[0]), cmul(s[1][e], e256[1], e2r[1]));
                    if (e == 4 || e == 5 || e == 7) {
#pragma unroll
                        for (int m = 0; m < 2; ++m) v[m] = cmul(e == 4 ? s[m][4] : v[m], e256[m], e2r[m]);
                        put(e == 4 ? 10 : e == 5 ? 13 : 15, v[0], v[1]);
                    }
                }
            }
            sub_arrive(cnt + 2, lane);
            STAMP(12);
            {
                const float cv = bflo(q_c);
                const float s = rbf(cv * sigmoidf_(cv));
                const f32x2 p0 = unbf(wmv.x) * s, p1 = unbf(wmv.y) * s, p2 = unbf(wmv.z) * s, p3 = unbf(wmv.w) * s;
                float a = swap16_add(swap32_add(p0.x, p2.x), swap32_add(p1.x, p3.x));
                float b = swap16_add(swap32_add(p0.y, p2.y), swap32_add(p1.y, p3.y));
                a += dppf<ROR8>(a); b += dppf<ROR8>(b);
                STWS((u32x4{__float_as_uint(a), tag, __float_as_uint(b), tag}), mpw_off);
            }
            __builtin_amdgcn_s_setprio(0);
            STAMP(2);
            __builtin_amdgcn_s_sleep(MPSLEEP);
            u32x4 xt[4], mp;
            for (u32 spins = 0;; ++spins) {
                MEMFENCE();
                mp = LDWS(mp_off);
#pragma unroll
                for (int k = 0; k < 4; ++k) xt[k] = LDWS(xt_off + k * (64 * 14336));
                bool ok = wave >= 6 || (mp.y == tag && mp.w == tag);
#pragma unroll
                for (int k = 0; k < 4; ++k) ok = ok && xt[k].y == tag && xt[k].w == tag;
                if (__all(ok) || spins > SPIN_CAP) break;
            }
            MEMFENCE();
            STAMP(5);
            STWS(ones, WS_Z + d0 * (2 * L_) + t4 * 16);
            STWS(ones, WS_Z + d0 * (2 * L_) + 4096 + t4 * 16);
            if (wave < 6) {
                float a = act56 ? __uint_as_float(mp.x) : 0.f, b = act56 ? __uint_as_float(mp.z) : 0.f;
                wave_sum2(a, b);
                if (lane == 0) *(LAS f32x2*)(misc + 16 + 2 * (wave & 1)) = rbf2(f32x2{a, b});
                sub_arrive(cnt + 1, lane);
            }
            sub_wait(cnt + 1, 2);
            STAMP(3);
            const float sa = misc[16 + hch], ca1 = rbf(misc[18 + hch] + 1.f), dsk = bflo(q_dsk);
            const int hexp = max(__builtin_amdgcn_frexp_expf(fmaf(66.f, fabsf(ca1), fabsf(sa) * 1.02f)) - 15, -126);
            const float hsc = pow2c(-hexp);
            if (tid == 256 || tid == 258) ((LAS int*)misc)[24 + hch] = hexp;
            {
                const int tb = (t4 >> 2) * 16 + (t4 & 1) * 8;
#pragma unroll
                for (int k = 0; k < 4; ++k) {
                    const f32x2 ha = rbf2(rbf2(unbf(xt[k].x) * ca1) + sa);
                    const f32x2 hb = rbf2(rbf2(unbf(xt[k].z) * ca1) + sa);
                    *(LAS u32x2*)(smem + LDS_HS + hch * HSCH + 512 + tb + k * 1024) = u32x2{pkh(ha * hsc), pkh(hb * hsc)};
                    *(LAS u32x2*)(smem + LDS_HD + hch * 4096 + tb + k * 1024) = u32x2{pkbf(ha * dsk), pkbf(hb * dsk)};
                }
            }
            __syncthreads();
            STAMP(6);
            if (bid == 0 && tid == 256) __hip_atomic_store((GAS u32*)(p.ws + WS_EPOCH), tag, RLX_AGENT);
            asm volatile("s_waitcnt vmcnt(0)" ::: "memory");
            if (lane == 0) __hip_atomic_store((GAS u32*)(p.ws + WS_PD) + pair * 4 + (wave - 4), tag, RLX_AGENT);
            *(volatile LAS u32*)(misc + 48 + wave) = 1u;
            build_vec();
            sub_arrive(cnt + 3, lane);
            STAMP(7);
            check_pd();
            sub_wait(cnt + 3, 4);
            STAMP(19);
        }
        STAMP(8);
    } else {
        const u32 q_c = p.c[t448];
        MEMFENCE(); SCHED();
        const int rg = t448 / 7, c7 = t448 - 7 * rg;
        LAS float* const sv = (LAS float*)(smem + LDS_KD);
        LAS float* const part = (LAS float*)(smem + LDS_HD);
        ln1_compute([] {}, [&] {
            const float cv = bflo(q_c);
            sv[t448] = rbf(cv * sigmoidf_(cv));
        });
        ln1_store();
        SCHED();
        STAMP(1);
        {
            u32x4 wmv[7];
#pragma unroll
            for (int i = 0; i < 7; ++i) wmv[i] = *(const GAS u32x4*)(p.w_mod + (rg + 64 * i) * (6 * D_) + 2 * D_ + 56 * sid + 8 * c7);
            MEMFENCE(); SCHED();
            f32x2 a[4] = {{0.f, 0.f}, {0.f, 0.f}, {0.f, 0.f}, {0.f, 0.f}};
#pragma unroll
            for (int i = 0; i < 7; ++i) {
                const float s = sv[rg + 64 * i];
#pragma unroll
                for (int e = 0; e < 4; ++e) { const u32 w = wmv[i][e]; a[e] = pfma(unbf(w), (f32x2)s, a[e]); }
            }
            LAS f32x4* pw = (LAS f32x4*)(part + (tid < D_ ? rg * 56 + 8 * c7 : 64 * 56));
            pw[0] = f32x4{a[0].x, a[0].y, a[1].x, a[1].y};
            pw[1] = f32x4{a[2].x, a[2].y, a[3].x, a[3].y};
        }
        __syncthreads();
        if (wave < 7) {
            const f32x4 lo = *(const LAS f32x4*)(part + lane * 56 + 8 * wave), hi = *(const LAS f32x4*)(part + lane * 56 + 8 * wave + 4);
            float l0 = lo.x, l1 = lo.y, l2 = lo.z, l3 = lo.w, h0 = hi.x, h1 = hi.y, h2 = hi.z, h3 = hi.w;
            wave_sum4(l0, l1, l2, l3); wave_sum4(h0, h1, h2, h3);
            const u32 o0 = pkbf(f32x2{l0, l1}), o1 = pkbf(f32x2{l2, l3}), o2 = pkbf(f32x2{h0, h1}), o3 = pkbf(f32x2{h2, h3});
            STWS((lane ? u32x4{o2, tag, o3, tag} : u32x4{o0, tag, o1, tag}), lane < 2 ? WS_MODB + (u32)((28 * sid + 4 * wave + 2 * lane) * 8) : WS_END);
        }
        STAMP(2);
        SCHED();
        __syncthreads();
        STAMP(5);
        if (wave < 4) {
            const int sw = min(sid, 27);
#pragma unroll
            for (int i = 0; i < 14; ++i) {
                const int cidx = tid + 256 * i, k = cidx >> 3, c8 = (cidx ^ (k >> 1)) & 7;
                __builtin_amdgcn_global_load_lds((const GAS void*)(p.w_gate + k * F_ + 64 * sw + 8 * c8),
                                                 (LAS void*)(smem + (wave + 4 * i) * 1024), 16, 0, 0);
                __builtin_amdgcn_global_load_lds((const GAS void*)(p.w_up + k * F_ + 64 * sw + 8 * (c8 ^ 2)),
                                                 (LAS void*)(smem + 57344 + (wave + 4 * i) * 1024), 16, 0, 0);
            }
            asm volatile("s_waitcnt vmcnt(0)" ::: "memory");
        } else {
            const int t4 = tid - 256;
#pragma unroll
            for (int i = 0; i < 4; ++i)
                STWS(ones, is_mlp && t4 + 256 * i < 896 ? WS_PART + (u32)(sid * 14336 + (t4 + 256 * i) * 16) : WS_END);
            build_vec();
        }
        __syncthreads();
        STAMP(4);
        {
            const int mt = (r16 & 3) >> 1, row = 8 * g4 + (r16 >> 2);
            const LAS unsigned char* s0 = smem + mt * 57344 + row * 128 + ((wave ^ (row >> 1) ^ (2 * mt)) & 7) * 16 + 8 * (r16 & 1);
            const LAS unsigned char* s1 = smem + mt * 57344 + (row + 4) * 128 + ((wave ^ ((row + 4) >> 1) ^ (2 * mt)) & 7) * 16 + 8 * (r16 & 1);
#pragma unroll
            for (int ks = 0; ks < 14; ++ks) {
                const u32x2 lo = __builtin_bit_cast(u32x2, __builtin_amdgcn_ds_read_tr16_b64_v4i16((LAS s16x4*)(s0 + ks * 4096)));
                const u32x2 hi = __builtin_bit_cast(u32x2, __builtin_amdgcn_ds_read_tr16_b64_v4i16((LAS s16x4*)(s1 + ks * 4096)));
                wgu[ks] = u32x4{lo.x, lo.y, hi.x, hi.y};
            }
        }
        {
            const int sw = min(sid, 27);
            u32x4 sd[7];
#pragma unroll
            for (int i = 0; i < 7; ++i) sd[i] = *(const GAS u32x4*)(p.w_down + 64 * sw * D_ + (tid + NT * i) * 8);
            __syncthreads();
#pragma unroll
            for (int i = 0; i < 7; ++i) {
                const int cidx = tid + NT * i, f = cidx / 56;
                *(LAS u32x4*)(smem + MLP_DN + f * 928 + (f >> 3) * 128 + (cidx - 56 * f) * 16) = sd[i];
            }
        }
        STAMP(3);
        if (wave >= 4) {
            for (u32 spins = 0;; ++spins) {
                MEMFENCE();
                const u32 w = __hip_atomic_load((GAS u32*)(p.ws + WS_PD) + sid * 28 + (wave - 4), RLX_AGENT);
                if (__builtin_amdgcn_readfirstlane(w) == tag || spins > SPIN_CAP) break;
                __builtin_amdgcn_s_sleep(16);
            }
            __builtin_amdgcn_s_sleep(WSLEEP);
            check_pd();
        }
        __syncthreads();
        STAMP(8);
    }

    const u32 zr_off = WS_Z + (u32)(t448 * (2 * L_) + tg * 16);
    STAMP(9);
    asm volatile("" :: "v"(pfc));
    const LAS f32x4* const vec = (const LAS f32x4*)(smem + LDS_VEC) + 2 * l56;
    f32x2 xv[4], gav[4], cw[4];
    float scw, ssw;
    {
        const f32x4 g0 = vec[0], g1 = vec[1], c0 = vec[112], c1 = vec[113];
        const f32x4 p0 = *(const LAS f32x4*)(misc + 40), p1 = *(const LAS f32x4*)(misc + 44);
        gav[0] = g0.xy; gav[1] = g0.zw; gav[2] = g1.xy; gav[3] = g1.zw;
        cw[0] = c0.xy; cw[1] = c0.zw; cw[2] = c1.xy; cw[3] = c1.zw;
#pragma unroll
        for (int i = 0; i < 4; ++i) { const u32 x = xrow[i]; xv[i] = unbf(x); }
        scw = (p0.x + p0.z) + (p1.x + p1.z); ssw = (p0.y + p0.w) + (p1.y + p1.w);
    }
    SCHED();
    asm volatile("s_waitcnt vmcnt(0)" ::: "memory");
    if (!is_conv && wave == 0) {
        for (u32 spins = 0;; ++spins) {
            MEMFENCE();
            const u32 w = __hip_atomic_load((GAS u32*)(p.ws + WS_Z + sid * 14 * (2 * L_) + tg * 16), RLX_AGENT);
            if (__builtin_amdgcn_readfirstlane(w) != ~0u || spins > SPIN_CAP) break;
            __builtin_amdgcn_s_sleep(4);
        }
    }
    __syncthreads();
    STAMP(22);
    u32x4 z;
    for (u32 spins = 0;; ++spins) {
        MEMFENCE();
        z = LDWS(zr_off);
        if (__all(umax4(z) != ~0u) || spins > SPIN_CAP) break;
        __builtin_amdgcn_s_sleep(1);
    }
    {
        LAS unsigned char* tr = smem + LDS_TR + t448 * 2;
#pragma unroll
        for (int i = 0; i < 4; ++i) {
            const u32 w = z[i];
            *(LAS u16*)(tr + (2 * i) * TRROW) = (u16)w;
            *(LAS u16*)(tr + (2 * i + 1) * TRROW) = (u16)(w >> 16);
        }
    }
    __syncthreads();
    STAMP(10);
    {
        const u32x4 yr = *(const LAS u32x4*)(smem + LDS_TR + wave * TRROW + l56 * 16);
        f32x2 v[4];
#pragma unroll
        for (int i = 0; i < 4; ++i) { const u32 b = yr[i]; v[i] = rbf2(xv[i] + rbf2(unbf(b) * gav[i])); }
        const float pv = __uint_as_float(__builtin_amdgcn_readfirstlane(__float_as_uint(v[0].x)));
        f32x2 s1 = {0.f, 0.f}, s2 = {0.f, 0.f}, s3 = {0.f, 0.f};
#pragma unroll
        for (int i = 0; i < 4; ++i) { const f32x2 dlt = v[i] - pv; s1 += dlt; s2 = pfma(dlt, dlt, s2); s3 = pfma(dlt, cw[i], s3); }
        float a = act56 ? s1.x + s1.y : 0.f, b = act56 ? s2.x + s2.y : 0.f, c = act56 ? s3.x + s3.y : 0.f, d = 0.f;
        wave_sum4(a, b, c, d);
        float m, rd;
        ln_finish(pv, a, b, m, rd);
        const float r = fmaf(fmaf(pv - m, scw, c), rd, ssw);
        if (lane == 0) {
            u32 rb = pkbf(f32x2{r, 0.f}) & 0xffffu;
            rb = rb == 0x8000u ? 0u : rb;
            *(LAS u16*)(smem + LDS_MISC + 512 + wave * 2) = (u16)((rb & 0x8000u) ? ~rb : (rb | 0x8000u));
        }
        __syncthreads();
        if (tid < 2) {
            const u32x2 rr = *(const LAS u32x2*)(smem + LDS_MISC + 512 + tid * 8);
            STWS((u32x4{rr.x, tag, rr.y, tag}), WS_RBUF + tg * 32 + tid * 16);
        }
        STAMP(18);
        const f32x4 cb0 = vec[224], cbb = vec[225], sb0 = vec[336], sbb = vec[337], gb0 = vec[448], gbb = vec[449];
        const f32x2 cb1[4] = {cb0.xy, cb0.zw, cbb.xy, cbb.zw}, sbv[4] = {sb0.xy, sb0.zw, sbb.xy, sbb.zw}, gbv[4] = {gb0.xy, gb0.zw, gbb.xy, gbb.zw};
        u32x4 hp, o;
#pragma unroll
        for (int i = 0; i < 4; ++i) {
            const u32 h = pkbf(rbf2(rbf2(rbf2(v[i] - m) * rd) * cb1[i]) + sbv[i]);
            hp[i] = h;
            o[i] = pkbf(v[i] + rbf2(unbf(h) * gbv[i]));
        }
        const __amdgpu_buffer_rsrc_t outr = __builtin_amdgcn_make_buffer_rsrc(p.out, 0, L_ * D_ * 2, 0x00020000);
        if (act56) {
            STWS(hp, WS_H2 + (town * D_ + lane * 8) * 2);
            __builtin_amdgcn_raw_buffer_store_b128(o, outr, (u32)((town * D_ + lane * 8) * 2), 0, 16);
        }
    }
    if (!is_mlp) {
        asm volatile("s_waitcnt vmcnt(0)" ::: "memory");
        __syncthreads();
        if (tid == 0) __hip_atomic_store((GAS u32*)(p.ws + WS_ODONE) + tg, tag, RLX_AGENT);
        STAMP(11);
        STAMP_DUMP();
        return;
    }
    STAMP(11);

    LAS u32* const top = (LAS u32*)(smem + MLP_TOP);
    LAS u32* const cand = (LAS u32*)(smem + MLP_CAND);
    if (tid < 64) cand[tid] = 0u;
    if (tid == 64) top[64] = 0u;
    u32x4 rb;
    for (u32 spins = 0;; ++spins) {
        MEMFENCE();
        rb = LDWS(WS_RBUF + tid * 16);
        if (__all(rb.y == tag && rb.w == tag) || spins > SPIN_CAP) break;
        __builtin_amdgcn_s_sleep(1);
    }
    MEMFENCE();
    STAMP(12);
    u32 key[4];
    {
        const u32 ib = (u32)(2048 - 4 * tid);
        key[0] = (rb.x << 16) | ib; key[1] = (rb.x & 0xffff0000u) | (ib - 1);
        key[2] = (rb.z << 16) | (ib - 2); key[3] = (rb.z & 0xffff0000u) | (ib - 3);
        const u32 cm = cls_umax(umax(umax(key[0], key[1]), umax(key[2], key[3])));
        if (lane < 8) top[wave * 8 + lane] = cm;
    }
    __syncthreads();
    if (tid == 0) __hip_atomic_store((GAS u32*)(p.ws + WS_ODONE) + tg, tag, RLX_AGENT);
    const u32x4 odn = LDWS(WS_ODONE + lane * 16);
    MEMFENCE();
    u32 mykey;
    {
        u32 thr = cls_umax(top[lane]);
        thr = umin(thr, dppu<XOR1>(thr)); thr = umin(thr, dppu<XOR2>(thr)); thr = umin(thr, dppu<HMIR>(thr));
        const u64 m0 = __ballot(key[0] >= thr), m1 = __ballot(key[1] >= thr), m2 = __ballot(key[2] >= thr), m3 = __ballot(key[3] >= thr);
        const u32 n0 = __builtin_popcountll(m0), n1 = __builtin_popcountll(m1), n2 = __builtin_popcountll(m2), n3 = __builtin_popcountll(m3);
        if (n0 + n1 + n2 + n3) {
            u32 base = 0;
            if (lane == 0) base = __hip_atomic_fetch_add(top + 64, n0 + n1 + n2 + n3, __ATOMIC_RELAXED, __HIP_MEMORY_SCOPE_WORKGROUP);
            u32 p0 = key[0] >= thr ? mbcnt64(m0) : 2048u, p1 = key[1] >= thr ? mbcnt64(m1) + n0 : 2048u;
            u32 p2 = key[2] >= thr ? mbcnt64(m2) + n0 + n1 : 2048u, p3 = key[3] >= thr ? mbcnt64(m3) + n0 + n1 + n2 : 2048u;
            asm volatile("" : "+v"(p0), "+v"(p1), "+v"(p2), "+v"(p3));
            LAS u32* const cb = cand + __builtin_amdgcn_readfirstlane(base);
            cb[p0] = key[0]; cb[p1] = key[1]; cb[p2] = key[2]; cb[p3] = key[3];
        }
        __syncthreads();
        const u32 cntv = top[64];
        u32 c = cand[lane & 31];
        u32x4 q[4];
#pragma unroll
        for (int e = 0; e < 4; ++e) q[e] = *(const LAS u32x4*)(cand + 16 * (lane >> 5) + 4 * e);
        const u32 cnt = __builtin_amdgcn_readfirstlane(cntv);
        if (cnt <= 64u) {
            mykey = 0;
            for (u32 cb = 0;;) {
                u32 rk = 0;
#pragma unroll
                for (int e = 0; e < 4; ++e) rk += (q[e].x > c) + (q[e].y > c) + (q[e].z > c) + (q[e].w > c);
                if (cnt > 32u) {
#pragma unroll
                    for (int e = 0; e < 4; ++e) {
                        const u32x4 q2 = *(const LAS u32x4*)(cand + 32 + 16 * (lane >> 5) + 4 * e);
                        rk += (q2.x > c) + (q2.y > c) + (q2.z > c) + (q2.w > c);
                    }
                }
                const auto s = __builtin_amdgcn_permlane32_swap(rk, rk, false, false);
                rk = s[0] + s[1];
                const u64 hit = __ballot(c && rk == (u32)wave);
                if (hit) mykey = __builtin_amdgcn_readlane(c, __builtin_ctzll(hit));
                cb += 32;
                if (cb >= cnt) break;
                c = cand[32 + (lane & 31)];
            }
        } else {
            mykey = 0;
#pragma unroll 1
            for (int it = 0; it < 8; ++it) {
                const u32 wm = wave_umax(umax(umax(key[0], key[1]), umax(key[2], key[3])));
                __syncthreads();
                if (lane == 0) top[wave] = wm;
                __syncthreads();
                const u32 w = umax(umax4(*(const LAS u32x4*)(top)), umax4(*(const LAS u32x4*)(top + 4)));
#pragma unroll
                for (int i = 0; i < 4; ++i) key[i] = key[i] == w ? 0u : key[i];
                mykey = it == wave ? w : mykey;
            }
        }
    }
    STAMP(13);
    {
        const int t = __builtin_amdgcn_readfirstlane(2048 - (int)(mykey & 0xffffu)) & 2047;
        const int e = (t >> 3) & 3;
        u32 f = __builtin_amdgcn_readlane(e == 0 ? odn.x : e == 1 ? odn.y : e == 2 ? odn.z : odn.w, t >> 5);
        for (u32 spins = 0; f != tag && spins <= SPIN_CAP; ++spins) {
            MEMFENCE();
            f = __builtin_amdgcn_readfirstlane(__hip_atomic_load((GAS u32*)(p.ws + WS_ODONE) + (t >> 3), RLX_AGENT));
        }
        MEMFENCE();
        const u32x4 hp = LDWS(WS_H2 + (t * D_ + l56 * 8) * 2);
        MEMFENCE();
    {
        const int w7 = wave < 7 ? wave : 6;
        const LAS unsigned char* src = smem + MLP_DN + (8 * g4 + (r16 >> 2)) * 928 + g4 * 128 + (64 * w7 + 4 * (r16 & 3)) * 2;
#pragma unroll
        for (int i = 0; i < 8; ++i) {
            const LAS unsigned char* q = src + (i & 1) * (32 * 928 + 4 * 128) + (i >> 1) * 32;
            const u32x2 lo = __builtin_bit_cast(u32x2, __builtin_amdgcn_ds_read_tr16_b64_v4i16((LAS s16x4*)(q)));
            const u32x2 hi = __builtin_bit_cast(u32x2, __builtin_amdgcn_ds_read_tr16_b64_v4i16((LAS s16x4*)(q + 4 * 928)));
            wdn[i] = u32x4{lo.x, lo.y, hi.x, hi.y};
        }
    }
        SCHED();
        if (lane == 0) top[72 + wave] = mykey;
        *(LAS u32x4*)(smem + MLP_H + wave * TRROW + l56 * 16) = hp;
    }
    __syncthreads();
    STAMP(14);
    {
        f32x4 acc = {0.f, 0.f, 0.f, 0.f};
        const LAS unsigned char* hb = smem + MLP_H + (r16 & 7) * TRROW + g4 * 16;
        bf16x8 hf[14];
#pragma unroll
        for (int ks = 0; ks < 14; ++ks) hf[ks] = *(const LAS bf16x8*)(hb + 64 * ks);
        LAS u16* const aw = (LAS u16*)(smem + MLP_ACT + (r16 & 7) * 144) + 8 * wave + 4 * (g4 & 1) + 2 * (r16 >> 3) + (lane >> 5);
        SCHED();
#pragma unroll
        for (int ks = 0; ks < 14; ++ks)
            acc = __builtin_amdgcn_mfma_f32_16x16x32_bf16(__builtin_bit_cast(bf16x8, wgu[ks]), hf[ks], acc, 0, 0, 0);
        const u32 mine = pkbf(r16 < 8 ? acc.xy : acc.zw);
        const auto s = __builtin_amdgcn_permlane32_swap(mine, mine, false, false);
        const float gv = lane < 32 ? bflo(s[0]) : bfhi(s[0]), uv = lane < 32 ? bflo(s[1]) : bfhi(s[1]);
        *aw = (u16)pkbf(f32x2{rbf(gv * sigmoidf_(gv)) * uv, 0.f});
    }
    __syncthreads();
    STAMP(15);
    if (wave < 7) {
        const LAS unsigned char* ab = smem + MLP_ACT + (r16 & 7) * 144 + g4 * 16;
        const bf16x8 a0 = *(const LAS bf16x8*)(ab), a1 = *(const LAS bf16x8*)(ab + 64);
        const u32 po = r16 < 8 ? WS_PART + (u32)(((sid * 28 + 4 * wave) * 8 + r16) * 64 + g4 * 16) : WS_END;
#pragma unroll
        for (int i = 0; i < 4; ++i) {
            f32x4 acc = {0.f, 0.f, 0.f, 0.f};
            acc = __builtin_amdgcn_mfma_f32_16x16x32_bf16(__builtin_bit_cast(bf16x8, wdn[2 * i]), a0, acc, 0, 0, 0);
            acc = __builtin_amdgcn_mfma_f32_16x16x32_bf16(__builtin_bit_cast(bf16x8, wdn[2 * i + 1]), a1, acc, 0, 0, 0);
            STWS(__builtin_bit_cast(u32x4, acc), po + 512u * i);
        }
        asm volatile("s_waitcnt vmcnt(0)" ::: "memory");
        STAMP(23);
        const int pp = tid >> 5, ck = tid & 31;
        u32x4 a, b;
        for (u32 spins = 0;; ++spins) {
            MEMFENCE();
            a = LDWS(WS_PART + ((2 * pp) * 28 + sid) * 512 + ck * 16);
            b = LDWS(WS_PART + ((2 * pp + 1) * 28 + sid) * 512 + ck * 16);
            if (__all(umax(umax4(a), umax4(b)) != ~0u) || spins > SPIN_CAP) break;
        }
        *(LAS f32x4*)(smem + MLP_RED + pp * 512 + ck * 16) = __builtin_bit_cast(f32x4, a) + __builtin_bit_cast(f32x4, b);
        STAMP(16);
        __syncthreads();
    } else {
        const int fj = lane >> 3, fc = 16 * sid + 2 * (lane & 7);
        const u32 fkey = top[72 + fj];
        const int ft = (2048 - (int)(fkey & 0xffffu)) & 2047;
        const u32 y0 = __hip_atomic_load((GAS u16*)(p.ws + WS_Z) + fc * L_ + ft, RLX_AGENT);
        const u32 y1 = __hip_atomic_load((GAS u16*)(p.ws + WS_Z) + (fc + 1) * L_ + ft, RLX_AGENT);
        const u32 xx = *(const GAS u32*)(p.x + ft * D_ + fc);
        const u32 ob = fkey >> 16;
        const float rs = rbf(sigmoidf_(bflo((ob & 0x8000u) ? (ob & 0x7fffu) : (~ob & 0xffffu))));
        const f32x2 h2 = unbf(*(const LAS u32*)(smem + MLP_H + fj * TRROW + fc * 2));
        const f32x2 ga = *(const LAS f32x2*)(smem + LDS_VEC + fc * 4), gb = *(const LAS f32x2*)(smem + LDS_VEC + 4 * 1792 + fc * 4);
        f32x2 x1 = rbf2(unbf(xx) + rbf2(f32x2{bflo(y0), bflo(y1)} * ga));
        asm volatile("" : "+v"(x1));
        __syncthreads();
        f32x2 sm = {0.f, 0.f};
#pragma unroll
        for (int w = 0; w < 14; ++w) sm += *(const LAS f32x2*)(smem + MLP_RED + w * 512 + lane * 8);
        const f32x2 y = rbf2(rbf2(sm) * rs);
        __hip_atomic_store((GAS u32*)(p.out + ft * D_ + fc), pkbf(x1 + rbf2(rbf2(h2 + y) * gb)), RLX_AGENT);
    }
    STAMP(17);
    STAMP_DUMP();
    asm volatile(".Lfb_code_end:");
}

extern "C" void kernel_launch(void* const* d_in, const int* in_sizes, int n_in, void* d_out, int out_size, void* d_ws,
                              size_t ws_size, hipStream_t stream) {
    Params p;
    p.x = (const u16*)d_in[0];
    p.a_re = (const u16*)d_in[1]; p.a_im = (const u16*)d_in[2];
    p.b_re = (const u16*)d_in[3]; p.b_im = (const u16*)d_in[4];
    p.c_re = (const u16*)d_in[5]; p.c_im = (const u16*)d_in[6];
    p.log_dt = (const u16*)d_in[7]; p.dskip = (const u16*)d_in[8];
    p.w_r = (const u16*)d_in[10]; p.w_gate = (const u16*)d_in[11]; p.w_up = (const u16*)d_in[12]; p.w_down = (const u16*)d_in[13];
    p.c = (const u16*)d_in[14]; p.w_mod = (const u16*)d_in[15];
    p.out = (u16*)d_out;
    p.ws = (unsigned char*)d_ws;
    fused_block<<<NWG, NT, 0, stream>>>(p);
}
```
